```python
import torch

def _softmax(x, axis=-1):
    return torch.softmax(x, dim=axis)
def _gelu(x):
    return torch.nn.functional.gelu(x, approximate="tanh")
def _silu(x):
    return torch.nn.functional.silu(x)
def _sigmoid(x):
    return torch.sigmoid(x)
def _layernorm(x, axis=-1, eps=1e-5):
    m = x.mean(dim=axis, keepdim=True)
    v = x.var(dim=axis, keepdim=True, unbiased=False)
    return (x - m) / torch.sqrt(v + eps)
def _scatter_add(tgt, idx, src, axis=0):
    return tgt.clone().index_add_(axis, idx, src)
def _segment_sum(src, seg, n):
    out = torch.zeros((n,) + src.shape[1:], dtype=src.dtype)
    return out.index_add_(0, seg, src)
def _pad(x, pw):
    flat = tuple(v for pair in reversed(pw) for v in pair)
    return torch.nn.functional.pad(x, flat)

kernel_name = 'gated_mix__gqa_block_cached__glu_gated_bilinear__decode'

def setup_inputs():

    _w = (lambda a: (lambda t: t.to(torch.bfloat16) if t.is_floating_point() else t)(torch.as_tensor(a)))
    def _A_setup():
        B, Sq, D = 1, 1, 640
        Hq, Hkv, Sk, Dh = 32, 32, 8192, 192
        assert Hq % Hkv == 0, (Hq, Hkv)
        return {
            "x": _w((lambda *s: torch.randn(*s, dtype=torch.bfloat16) * 0.05)(B, Sq, D)),
            "k_cache": _w((lambda *s: torch.randn(*s, dtype=torch.bfloat16) * 3.2)(B, Hkv, Sk, Dh)),
            "v_cache": _w((lambda *s: torch.randn(*s, dtype=torch.bfloat16) * 0.05)(B, Hkv, Sk, Dh)),
            "wq": _w((lambda *s: torch.randn(*s, dtype=torch.bfloat16) * 3.2)(D, Hq * Dh) * (D ** -0.5)),
            "wo": _w((lambda *s: torch.randn(*s, dtype=torch.bfloat16) * 0.05)(Hq * Dh, D) * ((Hq * Dh) ** -0.5)),
        }
    _A_in = _A_setup()

    def _B_setup():
        B, S, D, F = 1, 1, 640, 1536
        return {
            "x": _w((lambda *s: torch.randn(*s, dtype=torch.bfloat16) * 0.05)(B, S, D)),
            "w_gate": _w((lambda *s: torch.randn(*s, dtype=torch.bfloat16) * 0.2)(D, F) * (D ** -0.5)),
            "w_up": _w((lambda *s: torch.randn(*s, dtype=torch.bfloat16) * 0.2)(D, F) * (D ** -0.5)),
            "w_down": _w((lambda *s: torch.randn(*s, dtype=torch.bfloat16) * 0.2)(F, D) * (F ** -0.5)),
        }
    _B_in = _B_setup()

    return {
        "A_x": _A_in["x"],
        "A_k_cache": _A_in["k_cache"],
        "A_v_cache": _A_in["v_cache"],
        "A_wq": _A_in["wq"],
        "A_wo": _A_in["wo"],
        "B_x": _B_in["x"],
        "B_w_gate": _B_in["w_gate"],
        "B_w_up": _B_in["w_up"],
        "B_w_down": _B_in["w_down"],
        "gate_w": _w((lambda *s: torch.randn(*s, dtype=torch.bfloat16) * 0.05)(640) * 16.0),
    }

def reference(A_x, A_k_cache, A_v_cache, A_wq, A_wo, B_x, B_w_gate, B_w_up, B_w_down, gate_w):
    def _A_ref(x, k_cache, v_cache, wq, wo):
        B, Sq, D = x.shape
        Hq, Dh = 32, 192
        Hkv = k_cache.shape[1]
        rep = Hq // Hkv
        q = (lambda x, *d: x.permute(*d))((x @ wq).reshape(B, Sq, Hq, Dh), 0, 2, 1, 3)
        k = (lambda x, n, axis: torch.repeat_interleave(x, n, dim=axis))(k_cache, rep, axis=1)
        v = (lambda x, n, axis: torch.repeat_interleave(x, n, dim=axis))(v_cache, rep, axis=1)
        scores = torch.einsum("bhqd,bhkd->bhqk", q, k) * (Dh ** -0.5)
        p = _softmax(scores, axis=-1)
        o = torch.einsum("bhqk,bhkd->bhqd", p, v)
        return (lambda x, *d: x.permute(*d))(o, 0, 2, 1, 3).reshape(B, Sq, Hq * Dh) @ wo

    def _B_ref(x, w_gate, w_up, w_down):
        g = torch.einsum("bsd,df->bsf", x, w_gate)
        u = torch.einsum("bsd,df->bsf", x, w_up)
        h = g * u
        return torch.einsum("bsf,fd->bsd", h, w_down)

    x = A_x
    g = _sigmoid((x * gate_w).sum(-1, keepdims=True))
    a = _A_ref(x, A_k_cache, A_v_cache, A_wq, A_wo)
    b = _B_ref(x, B_w_gate, B_w_up, B_w_down)
    return g * a + (1 - g) * b
```

```cpp
#include <hip/hip_runtime.h>
#include <hip/hip_bf16.h>
#include <stdint.h>

#define RING 4
#define RING_PRO 1
#define NCTL 1
#define KV_AUX 2

namespace {

constexpr int D = 640, HQ = 32, SK = 8192, DH = 192, FF = 1536;
constexpr int NSPLIT = 8, NWG = 256, NT = 512, NW = 8;
constexpr int KEYS_WG = SK / NSPLIT;
constexpr int NB = KEYS_WG / 8 / NW;
constexpr int NQ = HQ * DH;
constexpr int CPW = D / NSPLIT;
constexpr int FPH = FF / HQ;

constexpr int NPC = DH + 2 * FPH;

constexpr size_t WS_EPOCH = 0;
constexpr int P1_STRIDE = 208;
constexpr size_t WS_P1 = 4096;
constexpr size_t WS_P2 = WS_P1 + (size_t)NWG * P1_STRIDE * 8;
constexpr size_t WS_PQ = WS_P2 + (size_t)NWG * CPW * 8;
constexpr size_t WS_END = WS_PQ + (size_t)NWG * NPC * 8;

constexpr int L_W = 0;
constexpr int L_QP = L_W + 5 * NT * 4;
constexpr int L_FP = L_QP + NW * DH;
constexpr int L_ML = L_FP + NW * 16;
constexpr int L_Q = L_ML + 16;
constexpr int L_G = L_Q + 96;
constexpr int L_Z = L_G + 4;
constexpr int L_PART = L_Z + 240;
constexpr int L_END = L_PART + 15 * NPC;

typedef unsigned u32x4 __attribute__((ext_vector_type(4)));
typedef float f32x4 __attribute__((ext_vector_type(4)));
typedef __bf16 bf16x2 __attribute__((ext_vector_type(2)));
typedef __attribute__((address_space(1))) unsigned long long gu64;
typedef __attribute__((address_space(1))) unsigned gu32;
#define RLX_AGENT __ATOMIC_RELAXED, __HIP_MEMORY_SCOPE_AGENT

__device__ __forceinline__ float bf_lo(unsigned w) { return __builtin_bit_cast(float, w << 16); }
__device__ __forceinline__ float bf_hi(unsigned w) { return __builtin_bit_cast(float, w & 0xffff0000u); }
__device__ __forceinline__ float bf_round(float v) { return (float)(__bf16)v; }
__device__ __forceinline__ unsigned bf_pack(float lo, float hi) {
  bf16x2 p; p[0] = (__bf16)lo; p[1] = (__bf16)hi; return __builtin_bit_cast(unsigned, p);
}
__device__ __forceinline__ float dot2(unsigned a, unsigned b, float c) {
  return __builtin_amdgcn_fdot2_f32_bf16(__builtin_bit_cast(bf16x2, a), __builtin_bit_cast(bf16x2, b), c, false);
}
template <int CTRL> __device__ __forceinline__ float dpp(float x) {
  return __builtin_bit_cast(float, __builtin_amdgcn_mov_dpp(__builtin_bit_cast(int, x), CTRL, 0xf, 0xf, true));
}
constexpr int XOR1 = 0xB1, XOR2 = 0x4E, HMIR = 0x141, ROR8 = 0x128, ROR4 = 0x124;

__device__ __forceinline__ float swap32_sum(float a, float b) {
  auto r = __builtin_amdgcn_permlane32_swap(__float_as_uint(a), __float_as_uint(b), false, false);
  return __uint_as_float(r[0]) + __uint_as_float(r[1]);
}
__device__ __forceinline__ float swap16_sum(float a, float b) {
  auto r = __builtin_amdgcn_permlane16_swap(__float_as_uint(a), __float_as_uint(b), false, false);
  return __uint_as_float(r[0]) + __uint_as_float(r[1]);
}
__device__ __forceinline__ float swap32_max(float a) {
  auto r = __builtin_amdgcn_permlane32_swap(__float_as_uint(a), __float_as_uint(a), false, false);
  return fmaxf(__uint_as_float(r[0]), __uint_as_float(r[1]));
}
__device__ __forceinline__ float swap16_max(float a) {
  auto r = __builtin_amdgcn_permlane16_swap(__float_as_uint(a), __float_as_uint(a), false, false);
  return fmaxf(__uint_as_float(r[0]), __uint_as_float(r[1]));
}
__device__ __forceinline__ void reduce24(const float (&v)[24], float (&o)[3], int g) {
  float a[12], b[6];
#pragma unroll
  for (int i = 0; i < 12; ++i) a[i] = swap32_sum(v[i], v[i + 12]);
#pragma unroll
  for (int i = 0; i < 6; ++i) b[i] = swap16_sum(a[i], a[i + 6]);
#pragma unroll
  for (int i = 0; i < 3; ++i) {
    const float lo = b[i] + dpp<ROR8>(b[i]), hi = b[i + 3] + dpp<ROR8>(b[i + 3]);
    o[i] = (g & 1) ? hi : lo;
  }
}
__device__ __forceinline__ void st_gran(unsigned char* ws, size_t off, unsigned tag, float v) {
  __hip_atomic_store((gu64*)(ws + off), ((unsigned long long)tag << 32) | __float_as_uint(v), RLX_AGENT);
}
__device__ __forceinline__ unsigned long long ld_gran(unsigned char* ws, size_t off) {
  return __hip_atomic_load((gu64*)(ws + off), RLX_AGENT);
}
__device__ __forceinline__ __amdgpu_buffer_rsrc_t mk_rsrc(const void* p, unsigned bytes) {
  return __builtin_amdgcn_make_buffer_rsrc((void*)p, 0, bytes, 0x00020000);
}
__device__ __forceinline__ u32x4 ldw(const unsigned short* p) {
  return __builtin_nontemporal_load(reinterpret_cast<const u32x4*>(p));
}
constexpr unsigned SPIN_CAP = 1u << 18;

__global__ void __launch_bounds__(NT, 2)
fused_decode(const unsigned short* __restrict__ x, const unsigned short* __restrict__ kc,
             const unsigned short* __restrict__ vc, const unsigned short* __restrict__ wq,
             const unsigned short* __restrict__ wo, const unsigned short* __restrict__ wg,
             const unsigned short* __restrict__ wu, const unsigned short* __restrict__ wd,
             const unsigned short* __restrict__ gw, unsigned short* __restrict__ out,
             unsigned char* ws) {
  __shared__ __attribute__((aligned(16))) float lds[L_END];
  const int bid = blockIdx.x;
  const int h = (bid & 7) * 4 + ((bid >> 3) & 3), s = bid >> 5;
  const int tid = threadIdx.x, lane = tid & 63, wave = __builtin_amdgcn_readfirstlane(tid >> 6);
  const int g = lane >> 3, c = lane & 7;

  unsigned long long ep = __hip_atomic_load((gu64*)(ws + WS_EPOCH), RLX_AGENT);

  const int prg = tid / 36, pch = tid - prg * 36;
  u32x4 wqv[6];
  unsigned short xq[6];
  {
    const unsigned short* pb = pch < 24 ? wq + h * DH + pch * 8
                             : (pch < 30 ? wg + h * FPH + (pch - 24) * 8 : wu + h * FPH + (pch - 30) * 8);
    const int ld = pch < 24 ? NQ : FF;
#pragma unroll
    for (int i = 0; i < 6; ++i) {
      const int r = prg + 14 * i;
      const int row = s * 80 + (r < 80 ? r : 0);
      wqv[i] = ldw(pb + (size_t)row * ld);
      xq[i] = x[row];
    }
  }
  __builtin_amdgcn_sched_barrier(0);
  __builtin_amdgcn_s_barrier();
  __builtin_amdgcn_sched_barrier(0);

  const size_t kvbase = (size_t)h * SK * DH;
  const __amdgpu_buffer_rsrc_t rk = mk_rsrc(kc + kvbase, SK * DH * 2);
  const __amdgpu_buffer_rsrc_t rv = mk_rsrc(vc + kvbase, SK * DH * 2);
  const int kvoff = s * 24576 + wave * 3072 + g * 384 + c * 16;
#define BSTR (8 * 24576)
  u32x4 kb[RING][3], vb[RING][3];
#define ISSUE(b_)                                                                                          \
  do {                                                                                                     \
    _Pragma("unroll") for (int j_ = 0; j_ < 3; ++j_)                                                       \
        kb[(b_) % RING][j_] = __builtin_amdgcn_raw_buffer_load_b128(rk, kvoff + j_ * 128, (b_) * BSTR, KV_AUX); \
    _Pragma("unroll") for (int j_ = 0; j_ < 3; ++j_)                                                       \
        vb[(b_) % RING][j_] = __builtin_amdgcn_raw_buffer_load_b128(rv, kvoff + j_ * 128, (b_) * BSTR, KV_AUX); \
    __builtin_amdgcn_sched_barrier(0);                                                                     \
  } while (0)
#pragma unroll
  for (int b = 0; b < RING_PRO; ++b) ISSUE(b);
  __builtin_amdgcn_sched_barrier(0);
  {
    float qa[8];
#pragma unroll
    for (int e = 0; e < 8; ++e) qa[e] = 0.f;
#pragma unroll
    for (int i = 0; i < 6; ++i) {
      const float xv = (prg + 14 * i < 80) ? bf_lo((unsigned)xq[i]) : 0.f;
#pragma unroll
      for (int k = 0; k < 4; ++k) {
        qa[2 * k] = fmaf(xv, bf_lo(wqv[i][k]), qa[2 * k]);
        qa[2 * k + 1] = fmaf(xv, bf_hi(wqv[i][k]), qa[2 * k + 1]);
      }
    }
    *reinterpret_cast<f32x4*>(&lds[L_PART + prg * NPC + pch * 8]) = f32x4{qa[0], qa[1], qa[2], qa[3]};
    *reinterpret_cast<f32x4*>(&lds[L_PART + prg * NPC + pch * 8 + 4]) = f32x4{qa[4], qa[5], qa[6], qa[7]};
  }
  __syncthreads();
  asm volatile("" : "+v"(ep));
  ep += 1ull;
  const unsigned tag = (unsigned)ep;
  const unsigned long long tag40 = ep & 0xffffffffffull;
  const size_t pqh = WS_PQ + (size_t)(h * NSPLIT) * NPC * 8;
  if (tid < NPC) {
    float v = 0.f;
#pragma unroll
    for (int r = 0; r < 14; ++r) v += lds[L_PART + r * NPC + tid];
    const unsigned vb24 = (__float_as_uint(v) + 0x80u) >> 8;
    __hip_atomic_store((gu64*)(ws + pqh + ((size_t)s * NPC + tid) * 8), (tag40 << 24) | vb24, RLX_AGENT);
  }
  __builtin_amdgcn_sched_barrier(0);
  u32x4 wsl[5];
  {
    const int rg = tid / 10, ch = tid - rg * 10;
#pragma unroll
    for (int i = 0; i < 5; ++i) {
      const int r = (tid < 480) ? rg * 5 + i : 0;
      const unsigned short* p = (r < DH) ? wo + (size_t)(h * DH + r) * D : wd + (size_t)(h * FPH + (r - DH)) * D;
      wsl[i] = ldw(p + s * CPW + ch * 8);
    }
  }
  unsigned gx[5], gg[5];
  if (wave == 7) {
#pragma unroll
    for (int i = 0; i < 5; ++i) {
      gx[i] = *reinterpret_cast<const unsigned*>(x + (i * 64 + lane) * 2);
      gg[i] = *reinterpret_cast<const unsigned*>(gw + (i * 64 + lane) * 2);
    }
  }
  __builtin_amdgcn_sched_barrier(0);
  if (wave >= NCTL) {
#pragma unroll
    for (int b = RING_PRO; b < RING; ++b) ISSUE(b);
  }
  __builtin_amdgcn_sched_barrier(0);

#pragma unroll
  for (int i = 0; i < 5; ++i) *reinterpret_cast<u32x4*>(&lds[L_W + (i * NT + tid) * 4]) = wsl[i];
  if (wave == 7) {
    float acc = 0.f;
#pragma unroll
    for (int i = 0; i < 5; ++i) {
      acc += bf_round(bf_lo(gx[i]) * bf_lo(gg[i]));
      acc += bf_round(bf_hi(gx[i]) * bf_hi(gg[i]));
    }
    acc += dpp<XOR1>(acc); acc += dpp<XOR2>(acc); acc += dpp<HMIR>(acc); acc += dpp<ROR8>(acc);
    acc = swap16_sum(acc, acc); acc = swap32_sum(acc, acc);
    const float z = bf_round(acc);
    const float sg = bf_round(1.f / (1.f + __expf(-z)));
    if (lane == 0) { lds[L_G] = sg; lds[L_G + 1] = bf_round(1.f - sg); }
  }
  if (wave < NCTL) {
    constexpr int NG = 3 / NCTL;
    unsigned long long gq[NG][8];
#pragma unroll 1
    for (unsigned spin = 0; spin < SPIN_CAP; ++spin) {
#pragma unroll
      for (int k = 0; k < NG; ++k)
#pragma unroll
        for (int i = 0; i < 8; ++i) gq[k][i] = ld_gran(ws, pqh + ((size_t)i * NPC + k * 64 + tid) * 8);
      bool ok = true;
#pragma unroll
      for (int k = 0; k < NG; ++k)
#pragma unroll
        for (int i = 0; i < 8; ++i) ok = ok & ((gq[k][i] >> 24) == tag40);
      if (__all(ok)) break;
    }
#pragma unroll
    for (int k = 0; k < NG; ++k) {
      float q = 0.f;
#pragma unroll
      for (int i = 0; i < 8; ++i) q += __uint_as_float(((unsigned)gq[k][i]) << 8);
      const float qn = dpp<XOR1>(q);
      if ((tid & 1) == 0) reinterpret_cast<unsigned*>(lds)[L_Q + ((k * 64 + tid) >> 1)] = bf_pack(q, qn);
    }
  }
  __syncthreads();
  if (wave < NCTL) {
#pragma unroll
    for (int b = RING_PRO; b < RING; ++b) ISSUE(b);
  }
  __builtin_amdgcn_sched_barrier(0);
  if (tid < 194) st_gran(ws, WS_P1 + ((size_t)(h * NSPLIT + s) * P1_STRIDE + tid) * 8, ~tag, 0.f);
  if (tid < 320 && (tid & 3) == 0) st_gran(ws, WS_P2 + ((size_t)(s * HQ + h) * CPW + (tid >> 2)) * 8, ~tag, 0.f);
  u32x4 qv[3];
#pragma unroll
  for (int j = 0; j < 3; ++j) qv[j] = *reinterpret_cast<const u32x4*>(&lds[L_Q + (c + 8 * j) * 4]);

  constexpr float C2 = 0.07216878364870322f * 1.4426950408889634f;
  float m = -INFINITY, l = 0.f, acc[24];
#pragma unroll
  for (int k = 0; k < 24; ++k) acc[k] = 0.f;
#pragma unroll
  for (int b = 0; b < NB; ++b) {
    const int sl = b % RING;
    float d0 = 0.f, d1 = 0.f, d2 = 0.f;
#pragma unroll
    for (int k = 0; k < 4; ++k) {
      d0 = dot2(kb[sl][0][k], qv[0][k], d0);
      d1 = dot2(kb[sl][1][k], qv[1][k], d1);
      d2 = dot2(kb[sl][2][k], qv[2][k], d2);
    }
    float sc = d0 + d1 + d2;
    sc += dpp<XOR1>(sc); sc += dpp<XOR2>(sc); sc += dpp<HMIR>(sc);
    sc *= C2;
    const float mn = fmaxf(m, sc);
    const float alpha = __builtin_amdgcn_exp2f(m - mn), p = __builtin_amdgcn_exp2f(sc - mn);
    m = mn;
    l = fmaf(l, alpha, p);
#pragma unroll
    for (int j = 0; j < 3; ++j)
#pragma unroll
      for (int k = 0; k < 4; ++k) {
        acc[j * 8 + 2 * k] = fmaf(p, bf_lo(vb[sl][j][k]), acc[j * 8 + 2 * k] * alpha);
        acc[j * 8 + 2 * k + 1] = fmaf(p, bf_hi(vb[sl][j][k]), acc[j * 8 + 2 * k + 1] * alpha);
      }
#pragma unroll
    for (int k = 0; k < 24; k += 4) asm volatile("" : "+v"(acc[k]), "+v"(acc[k + 1]), "+v"(acc[k + 2]), "+v"(acc[k + 3]));
    __builtin_amdgcn_sched_barrier(0);
    if (b + RING < NB) ISSUE(b + RING);
    __builtin_amdgcn_sched_barrier(0);
  }
#undef ISSUE
#undef BSTR

  {
    float M = fmaxf(m, dpp<ROR8>(m));
    M = swap16_max(M); M = swap32_max(M);
    const float f = __builtin_amdgcn_exp2f(m - M);
    l *= f;
#pragma unroll
    for (int k = 0; k < 24; ++k) acc[k] *= f;
    float o3[3];
    reduce24(acc, o3, g);
    l += dpp<ROR8>(l); l = swap16_sum(l, l); l = swap32_sum(l, l);
#pragma unroll
    for (int i = 0; i < 3; ++i) {
      const int a = 3 * g + i;
      lds[L_QP + wave * DH + (a >> 3) * 64 + c * 8 + (a & 7)] = o3[i];
    }
    if (lane == 0) { lds[L_ML + wave * 2] = M; lds[L_ML + wave * 2 + 1] = l; }
  }
#pragma unroll
  for (int i = 0; i < 5; ++i) wsl[i] = *reinterpret_cast<const u32x4*>(&lds[L_W + (i * NT + tid) * 4]);
  __syncthreads();
  const size_t p1h = WS_P1 + (size_t)(h * NSPLIT) * P1_STRIDE * 8;
  if (wave < 4) {
    if (tid < 194) {
      float M = -INFINITY;
#pragma unroll
      for (int w = 0; w < NW; ++w) M = fmaxf(M, lds[L_ML + 2 * w]);
      float o = 0.f, L = 0.f;
      const int t = tid < DH ? tid : 0;
#pragma unroll
      for (int w = 0; w < NW; ++w) {
        const float f = __builtin_amdgcn_exp2f(lds[L_ML + 2 * w] - M);
        o = fmaf(f, lds[L_QP + w * DH + t], o);
        L = fmaf(f, lds[L_ML + 2 * w + 1], L);
      }
      const float v = tid < DH ? o : (tid == DH ? M : L);
      st_gran(ws, p1h + ((size_t)s * P1_STRIDE + tid) * 8, tag, v);
    }
    const float gate = lds[L_G], gate1 = lds[L_G + 1];
    if (wave < 3) {
      unsigned long long ga[8], gm = 0;
      bool ok[8], okm = false;
#pragma unroll
      for (int i = 0; i < 8; ++i) { ok[i] = false; ga[i] = 0; }
      for (unsigned spin = 0; spin < SPIN_CAP; ++spin) {
#pragma unroll
        for (int i = 0; i < 8; ++i)
          if (!ok[i]) ga[i] = ld_gran(ws, p1h + ((size_t)i * P1_STRIDE + tid) * 8);
        if (!okm) gm = ld_gran(ws, p1h + ((size_t)(lane & 7) * P1_STRIDE + DH + ((lane >> 3) & 1)) * 8);
        bool all = true;
#pragma unroll
        for (int i = 0; i < 8; ++i) { ok[i] = __all((unsigned)(ga[i] >> 32) == tag); all = all && ok[i]; }
        okm = __all((unsigned)(gm >> 32) == tag);
        if (all && okm) break;
        __builtin_amdgcn_s_sleep(1);
      }
      const float val = __uint_as_float((unsigned)gm);
      const float other = dpp<ROR8>(val);
      const float ms = (lane & 8) ? other : val, ls = (lane & 8) ? val : other;
      float M = fmaxf(ms, dpp<XOR1>(ms)); M = fmaxf(M, dpp<XOR2>(M)); M = fmaxf(M, dpp<HMIR>(M));
      const float wgt = __builtin_amdgcn_exp2f(ms - M);
      float L = wgt * ls; L += dpp<XOR1>(L); L += dpp<XOR2>(L); L += dpp<HMIR>(L);
      const float wn = wgt / L;
      float o = 0.f;
#pragma unroll
      for (int i = 0; i < 8; ++i)
        o = fmaf(__builtin_bit_cast(float, __builtin_amdgcn_readlane(__builtin_bit_cast(int, wn), i)),
                 __uint_as_float((unsigned)ga[i]), o);
      lds[L_Z + tid] = gate * bf_round(o);
    } else {
      const int t = lane < FPH ? lane : 0;
      unsigned long long gh[16];
#pragma unroll 1
      for (unsigned spin = 0; spin < SPIN_CAP; ++spin) {
#pragma unroll
        for (int i = 0; i < 8; ++i) {
          gh[2 * i] = ld_gran(ws, pqh + ((size_t)i * NPC + DH + t) * 8);
          gh[2 * i + 1] = ld_gran(ws, pqh + ((size_t)i * NPC + DH + FPH + t) * 8);
        }
        bool ok = true;
#pragma unroll
        for (int i = 0; i < 16; ++i) ok = ok & ((gh[i] >> 24) == tag40);
        if (__all(ok)) break;
        __builtin_amdgcn_s_sleep(1);
      }
      float gs = 0.f, us = 0.f;
#pragma unroll
      for (int i = 0; i < 8; ++i) {
        gs += __uint_as_float(((unsigned)gh[2 * i]) << 8);
        us += __uint_as_float(((unsigned)gh[2 * i + 1]) << 8);
      }
      const float hh = bf_round(bf_round(gs) * bf_round(us));
      if (lane < FPH) lds[L_Z + DH + lane] = gate1 * hh;
    }
  }
  __syncthreads();
  if (tid < 480) {
    const int rg = tid / 10, ch = tid - rg * 10;
    float y[8];
#pragma unroll
    for (int e = 0; e < 8; ++e) y[e] = 0.f;
#pragma unroll
    for (int i = 0; i < 5; ++i) {
      const float z = lds[L_Z + rg * 5 + i];
#pragma unroll
      for (int k = 0; k < 4; ++k) {
        y[2 * k] = fmaf(z, bf_lo(wsl[i][k]), y[2 * k]);
        y[2 * k + 1] = fmaf(z, bf_hi(wsl[i][k]), y[2 * k + 1]);
      }
    }
    *reinterpret_cast<f32x4*>(&lds[L_PART + rg * 88 + ch * 8]) = f32x4{y[0], y[1], y[2], y[3]};
    *reinterpret_cast<f32x4*>(&lds[L_PART + rg * 88 + ch * 8 + 4]) = f32x4{y[4], y[5], y[6], y[7]};
  }
  __syncthreads();
  if (tid < 320) {
    const int col = tid >> 2, qt = tid & 3;
    float y = 0.f;
#pragma unroll
    for (int k = 0; k < 12; ++k) y += lds[L_PART + (4 * k + qt) * 88 + col];
    y += dpp<XOR1>(y); y += dpp<XOR2>(y);
    if (qt == 0) st_gran(ws, WS_P2 + ((size_t)(s * HQ + h) * CPW + col) * 8, tag, y);
  }
  if (wave == 7 && h < 20) {
    const int hh = lane >> 1, cp = (lane & 1) * 2;
    const size_t base = WS_P2 + ((size_t)(s * HQ + hh) * CPW + h * 4 + cp) * 8;
    unsigned long long g0 = 0, g1 = 0;
    for (unsigned spin = 0; spin < SPIN_CAP; ++spin) {
      g0 = ld_gran(ws, base); g1 = ld_gran(ws, base + 8);
      if (__all((unsigned)(g0 >> 32) == tag && (unsigned)(g1 >> 32) == tag)) break;
      __builtin_amdgcn_s_sleep(1);
    }
    float y0 = __uint_as_float((unsigned)g0), y1 = __uint_as_float((unsigned)g1);
    y0 += dpp<XOR2>(y0); y1 += dpp<XOR2>(y1);
    y0 += dpp<XOR1>(dpp<HMIR>(y0)); y1 += dpp<XOR1>(dpp<HMIR>(y1));
    y0 += dpp<ROR8>(y0); y1 += dpp<ROR8>(y1);
    y0 = swap16_sum(y0, y0); y1 = swap16_sum(y1, y1);
    y0 = swap32_sum(y0, y0); y1 = swap32_sum(y1, y1);
    if (lane < 2) *reinterpret_cast<unsigned*>(out + s * CPW + h * 4 + cp) = bf_pack(y0, y1);
    if (lane == 0 && h == 0 && s == 0) __hip_atomic_store((gu64*)(ws + WS_EPOCH), ep, RLX_AGENT);
  }
}

}

extern "C" void kernel_launch(void* const* d_in, const int* in_sizes, int n_in,
                              void* d_out, int out_size, void* d_ws, size_t ws_size,
                              hipStream_t stream) {
  (void)in_sizes; (void)n_in; (void)out_size; (void)ws_size;
  fused_decode<<<dim3(NWG), dim3(NT), 0, stream>>>(
      (const unsigned short*)d_in[0], (const unsigned short*)d_in[1], (const unsigned short*)d_in[2],
      (const unsigned short*)d_in[3], (const unsigned short*)d_in[4], (const unsigned short*)d_in[6],
      (const unsigned short*)d_in[7], (const unsigned short*)d_in[8], (const unsigned short*)d_in[9],
      (unsigned short*)d_out, (unsigned char*)d_ws);
}
```

```cpp
#define DEFMASK 57
#include <hip/hip_runtime.h>
#include <hip/hip_bf16.h>
#include <stdint.h>

#define RING 4
#define RING_PRO 1
#define NCTL 1
#define AX(j) ((((DEFMASK) >> (j)) & 1) ? 0 : 2)

namespace {

constexpr int D = 640, HQ = 32, SK = 8192, DH = 192, FF = 1536;
constexpr int NSPLIT = 8, NWG = 256, NT = 512, NW = 8;
constexpr int KEYS_WG = SK / NSPLIT;
constexpr int NB = KEYS_WG / 8 / NW;
constexpr int NQ = HQ * DH;
constexpr int CPW = D / NSPLIT;
constexpr int FPH = FF / HQ;

constexpr int NPC = DH + 2 * FPH;

constexpr size_t WS_EPOCH = 0;
constexpr int P1_STRIDE = 208;
constexpr size_t WS_P1 = 4096;
constexpr size_t WS_P2 = WS_P1 + (size_t)NWG * P1_STRIDE * 8;
constexpr size_t WS_PQ = WS_P2 + (size_t)NWG * CPW * 8;
constexpr size_t WS_END = WS_PQ + (size_t)NWG * NPC * 8;

constexpr int L_W = 0;
constexpr int L_QP = L_W + 5 * NT * 4;
constexpr int L_FP = L_QP + NW * DH;
constexpr int L_ML = L_FP + NW * 16;
constexpr int L_Q = L_ML + 16;
constexpr int L_G = L_Q + 96;
constexpr int L_Z = L_G + 4;
constexpr int L_PART = L_Z + 240;
constexpr int L_END = L_PART + 15 * NPC;

typedef unsigned u32x4 __attribute__((ext_vector_type(4)));
typedef float f32x4 __attribute__((ext_vector_type(4)));
typedef __bf16 bf16x2 __attribute__((ext_vector_type(2)));
typedef __attribute__((address_space(1))) unsigned long long gu64;
typedef __attribute__((address_space(1))) unsigned gu32;
#define RLX_AGENT __ATOMIC_RELAXED, __HIP_MEMORY_SCOPE_AGENT

__device__ __forceinline__ float bf_lo(unsigned w) { return __builtin_bit_cast(float, w << 16); }
__device__ __forceinline__ float bf_hi(unsigned w) { return __builtin_bit_cast(float, w & 0xffff0000u); }
__device__ __forceinline__ float bf_round(float v) { return (float)(__bf16)v; }
__device__ __forceinline__ unsigned bf_pack(float lo, float hi) {
  bf16x2 p; p[0] = (__bf16)lo; p[1] = (__bf16)hi; return __builtin_bit_cast(unsigned, p);
}
__device__ __forceinline__ float dot2(unsigned a, unsigned b, float c) {
  return __builtin_amdgcn_fdot2_f32_bf16(__builtin_bit_cast(bf16x2, a), __builtin_bit_cast(bf16x2, b), c, false);
}
template <int CTRL> __device__ __forceinline__ float dpp(float x) {
  return __builtin_bit_cast(float, __builtin_amdgcn_mov_dpp(__builtin_bit_cast(int, x), CTRL, 0xf, 0xf, true));
}
constexpr int XOR1 = 0xB1, XOR2 = 0x4E, HMIR = 0x141, ROR8 = 0x128, ROR4 = 0x124;

__device__ __forceinline__ float swap32_sum(float a, float b) {
  auto r = __builtin_amdgcn_permlane32_swap(__float_as_uint(a), __float_as_uint(b), false, false);
  return __uint_as_float(r[0]) + __uint_as_float(r[1]);
}
__device__ __forceinline__ float swap16_sum(float a, float b) {
  auto r = __builtin_amdgcn_permlane16_swap(__float_as_uint(a), __float_as_uint(b), false, false);
  return __uint_as_float(r[0]) + __uint_as_float(r[1]);
}
__device__ __forceinline__ float swap32_max(float a) {
  auto r = __builtin_amdgcn_permlane32_swap(__float_as_uint(a), __float_as_uint(a), false, false);
  return fmaxf(__uint_as_float(r[0]), __uint_as_float(r[1]));
}
__device__ __forceinline__ float swap16_max(float a) {
  auto r = __builtin_amdgcn_permlane16_swap(__float_as_uint(a), __float_as_uint(a), false, false);
  return fmaxf(__uint_as_float(r[0]), __uint_as_float(r[1]));
}
__device__ __forceinline__ void reduce24(const float (&v)[24], float (&o)[3], int g) {
  float a[12], b[6];
#pragma unroll
  for (int i = 0; i < 12; ++i) a[i] = swap32_sum(v[i], v[i + 12]);
#pragma unroll
  for (int i = 0; i < 6; ++i) b[i] = swap16_sum(a[i], a[i + 6]);
#pragma unroll
  for (int i = 0; i < 3; ++i) {
    const float lo = b[i] + dpp<ROR8>(b[i]), hi = b[i + 3] + dpp<ROR8>(b[i + 3]);
    o[i] = (g & 1) ? hi : lo;
  }
}
__device__ __forceinline__ void st_gran(unsigned char* ws, size_t off, unsigned tag, float v) {
  __hip_atomic_store((gu64*)(ws + off), ((unsigned long long)tag << 32) | __float_as_uint(v), RLX_AGENT);
}
__device__ __forceinline__ unsigned long long ld_gran(unsigned char* ws, size_t off) {
  return __hip_atomic_load((gu64*)(ws + off), RLX_AGENT);
}
__device__ __forceinline__ __amdgpu_buffer_rsrc_t mk_rsrc(const void* p, unsigned bytes) {
  return __builtin_amdgcn_make_buffer_rsrc((void*)p, 0, bytes, 0x00020000);
}
__device__ __forceinline__ u32x4 ldw(const unsigned short* p) {
  return __builtin_nontemporal_load(reinterpret_cast<const u32x4*>(p));
}
constexpr unsigned SPIN_CAP = 1u << 18;

__global__ void __launch_bounds__(NT, 2)
fused_decode(const unsigned short* __restrict__ x, const unsigned short* __restrict__ kc,
             const unsigned short* __restrict__ vc, const unsigned short* __restrict__ wq,
             const unsigned short* __restrict__ wo, const unsigned short* __restrict__ wg,
             const unsigned short* __restrict__ wu, const unsigned short* __restrict__ wd,
             const unsigned short* __restrict__ gw, unsigned short* __restrict__ out,
             unsigned char* ws) {
  __shared__ __attribute__((aligned(16))) float lds[L_END];
  const int bid = blockIdx.x;
  const int h = (bid & 7) * 4 + ((bid >> 3) & 3), s = bid >> 5;
  const int tid = threadIdx.x, lane = tid & 63, wave = __builtin_amdgcn_readfirstlane(tid >> 6);
  const int g = lane >> 3, c = lane & 7;

  unsigned long long ep = __hip_atomic_load((gu64*)(ws + WS_EPOCH), RLX_AGENT);

  const int prg = tid / 36, pch = tid - prg * 36;
  u32x4 wqv[6];
  unsigned short xq[6];
  {
    const unsigned short* pb = pch < 24 ? wq + h * DH + pch * 8
                             : (pch < 30 ? wg + h * FPH + (pch - 24) * 8 : wu + h * FPH + (pch - 30) * 8);
    const int ld = pch < 24 ? NQ : FF;
#pragma unroll
    for (int i = 0; i < 6; ++i) {
      const int r = prg + 14 * i;
      const int row = s * 80 + (r < 80 ? r : 0);
      wqv[i] = ldw(pb + (size_t)row * ld);
      xq[i] = x[row];
    }
  }
  __builtin_amdgcn_sched_barrier(0);
  __builtin_amdgcn_s_barrier();
  __builtin_amdgcn_sched_barrier(0);

  const size_t kvbase = (size_t)h * SK * DH;
  const __amdgpu_buffer_rsrc_t rk = mk_rsrc(kc + kvbase, SK * DH * 2);
  const __amdgpu_buffer_rsrc_t rv = mk_rsrc(vc + kvbase, SK * DH * 2);
  const int kvoff = s * 24576 + wave * 3072 + g * 384 + c * 16;
#define BSTR (8 * 24576)
  u32x4 kb[RING][3], vb[RING][3];
#define LD(dst, r, j, b_, bit) dst[(b_) % RING][j] = __builtin_amdgcn_raw_buffer_load_b128(r, kvoff + j * 128, (b_) * BSTR, AX(bit))
#define ISSUE(b_)                                                                \
  do {                                                                           \
    LD(kb, rk, 0, b_, 0); LD(kb, rk, 1, b_, 1); LD(kb, rk, 2, b_, 2);            \
    LD(vb, rv, 0, b_, 3); LD(vb, rv, 1, b_, 4); LD(vb, rv, 2, b_, 5);            \
    __builtin_amdgcn_sched_barrier(0);                                           \
  } while (0)
#pragma unroll
  for (int b = 0; b < RING_PRO; ++b) ISSUE(b);
  __builtin_amdgcn_sched_barrier(0);
  {
    float qa[8];
#pragma unroll
    for (int e = 0; e < 8; ++e) qa[e] = 0.f;
#pragma unroll
    for (int i = 0; i < 6; ++i) {
      const float xv = (prg + 14 * i < 80) ? bf_lo((unsigned)xq[i]) : 0.f;
#pragma unroll
      for (int k = 0; k < 4; ++k) {
        qa[2 * k] = fmaf(xv, bf_lo(wqv[i][k]), qa[2 * k]);
        qa[2 * k + 1] = fmaf(xv, bf_hi(wqv[i][k]), qa[2 * k + 1]);
      }
    }
    *reinterpret_cast<f32x4*>(&lds[L_PART + prg * NPC + pch * 8]) = f32x4{qa[0], qa[1], qa[2], qa[3]};
    *reinterpret_cast<f32x4*>(&lds[L_PART + prg * NPC + pch * 8 + 4]) = f32x4{qa[4], qa[5], qa[6], qa[7]};
  }
  __syncthreads();
  asm volatile("" : "+v"(ep));
  ep += 1ull;
  const unsigned tag = (unsigned)ep;
  const unsigned long long tag40 = ep & 0xffffffffffull;
  const size_t pqh = WS_PQ + (size_t)(h * NSPLIT) * NPC * 8;
  if (tid < NPC) {
    float v = 0.f;
#pragma unroll
    for (int r = 0; r < 14; ++r) v += lds[L_PART + r * NPC + tid];
    const unsigned vb24 = (__float_as_uint(v) + 0x80u) >> 8;
    __hip_atomic_store((gu64*)(ws + pqh + ((size_t)s * NPC + tid) * 8), (tag40 << 24) | vb24, RLX_AGENT);
  }
  __builtin_amdgcn_sched_barrier(0);
  u32x4 wsl[5];
  {
    const int rg = tid / 10, ch = tid - rg * 10;
#pragma unroll
    for (int i = 0; i < 5; ++i) {
      const int r = (tid < 480) ? rg * 5 + i : 0;
      const unsigned short* p = (r < DH) ? wo + (size_t)(h * DH + r) * D : wd + (size_t)(h * FPH + (r - DH)) * D;
      wsl[i] = ldw(p + s * CPW + ch * 8);
    }
  }
  unsigned gx[5], gg[5];
  if (wave == 7) {
#pragma unroll
    for (int i = 0; i < 5; ++i) {
      gx[i] = *reinterpret_cast<const unsigned*>(x + (i * 64 + lane) * 2);
      gg[i] = *reinterpret_cast<const unsigned*>(gw + (i * 64 + lane) * 2);
    }
  }
  __builtin_amdgcn_sched_barrier(0);
  if (wave >= NCTL) {
#pragma unroll
    for (int b = RING_PRO; b < RING; ++b) ISSUE(b);
  }
  __builtin_amdgcn_sched_barrier(0);

#pragma unroll
  for (int i = 0; i < 5; ++i) *reinterpret_cast<u32x4*>(&lds[L_W + (i * NT + tid) * 4]) = wsl[i];
  if (wave == 7) {
    float acc = 0.f;
#pragma unroll
    for (int i = 0; i < 5; ++i) {
      acc += bf_round(bf_lo(gx[i]) * bf_lo(gg[i]));
      acc += bf_round(bf_hi(gx[i]) * bf_hi(gg[i]));
    }
    acc += dpp<XOR1>(acc); acc += dpp<XOR2>(acc); acc += dpp<HMIR>(acc); acc += dpp<ROR8>(acc);
    acc = swap16_sum(acc, acc); acc = swap32_sum(acc, acc);
    const float z = bf_round(acc);
    const float sg = bf_round(1.f / (1.f + __expf(-z)));
    if (lane == 0) { lds[L_G] = sg; lds[L_G + 1] = bf_round(1.f - sg); }
  }
  if (wave < NCTL) {
    constexpr int NG = 3 / NCTL;
    unsigned long long gq[NG][8];
#pragma unroll 1
    for (unsigned spin = 0; spin < SPIN_CAP; ++spin) {
#pragma unroll
      for (int k = 0; k < NG; ++k)
#pragma unroll
        for (int i = 0; i < 8; ++i) gq[k][i] = ld_gran(ws, pqh + ((size_t)i * NPC + k * 64 + tid) * 8);
      bool ok = true;
#pragma unroll
      for (int k = 0; k < NG; ++k)
#pragma unroll
        for (int i = 0; i < 8; ++i) ok = ok & ((gq[k][i] >> 24) == tag40);
      if (__all(ok)) break;
    }
#pragma unroll
    for (int k = 0; k < NG; ++k) {
      float q = 0.f;
#pragma unroll
      for (int i = 0; i < 8; ++i) q += __uint_as_float(((unsigned)gq[k][i]) << 8);
      const float qn = dpp<XOR1>(q);
      if ((tid & 1) == 0) reinterpret_cast<unsigned*>(lds)[L_Q + ((k * 64 + tid) >> 1)] = bf_pack(q, qn);
    }
  }
  __syncthreads();
  if (wave < NCTL) {
#pragma unroll
    for (int b = RING_PRO; b < RING; ++b) ISSUE(b);
  }
  __builtin_amdgcn_sched_barrier(0);
  if (tid < 194) st_gran(ws, WS_P1 + ((size_t)(h * NSPLIT + s) * P1_STRIDE + tid) * 8, ~tag, 0.f);
  if (tid < 320 && (tid & 3) == 0) st_gran(ws, WS_P2 + ((size_t)(s * HQ + h) * CPW + (tid >> 2)) * 8, ~tag, 0.f);
  u32x4 qv[3];
#pragma unroll
  for (int j = 0; j < 3; ++j) qv[j] = *reinterpret_cast<const u32x4*>(&lds[L_Q + (c + 8 * j) * 4]);

  constexpr float C2 = 0.07216878364870322f * 1.4426950408889634f;
  float m = -INFINITY, l = 0.f, acc[24];
#pragma unroll
  for (int k = 0; k < 24; ++k) acc[k] = 0.f;
#pragma unroll
  for (int b = 0; b < NB; ++b) {
    const int sl = b % RING;
    float d0 = 0.f, d1 = 0.f, d2 = 0.f;
#pragma unroll
    for (int k = 0; k < 4; ++k) {
      d0 = dot2(kb[sl][0][k], qv[0][k], d0);
      d1 = dot2(kb[sl][1][k], qv[1][k], d1);
      d2 = dot2(kb[sl][2][k], qv[2][k], d2);
    }
    float sc = d0 + d1 + d2;
    sc += dpp<XOR1>(sc); sc += dpp<XOR2>(sc); sc += dpp<HMIR>(sc);
    sc *= C2;
    const float mn = fmaxf(m, sc);
    const float alpha = __builtin_amdgcn_exp2f(m - mn), p = __builtin_amdgcn_exp2f(sc - mn);
    m = mn;
    l = fmaf(l, alpha, p);
#pragma unroll
    for (int j = 0; j < 3; ++j)
#pragma unroll
      for (int k = 0; k < 4; ++k) {
        acc[j * 8 + 2 * k] = fmaf(p, bf_lo(vb[sl][j][k]), acc[j * 8 + 2 * k] * alpha);
        acc[j * 8 + 2 * k + 1] = fmaf(p, bf_hi(vb[sl][j][k]), acc[j * 8 + 2 * k + 1] * alpha);
      }
#pragma unroll
    for (int k = 0; k < 24; k += 4) asm volatile("" : "+v"(acc[k]), "+v"(acc[k + 1]), "+v"(acc[k + 2]), "+v"(acc[k + 3]));
    __builtin_amdgcn_sched_barrier(0);
    if (b + RING < NB) ISSUE(b + RING);
    __builtin_amdgcn_sched_barrier(0);
  }
#undef ISSUE
#undef BSTR

  {
    float M = fmaxf(m, dpp<ROR8>(m));
    M = swap16_max(M); M = swap32_max(M);
    const float f = __builtin_amdgcn_exp2f(m - M);
    l *= f;
#pragma unroll
    for (int k = 0; k < 24; ++k) acc[k] *= f;
    float o3[3];
    reduce24(acc, o3, g);
    l += dpp<ROR8>(l); l = swap16_sum(l, l); l = swap32_sum(l, l);
#pragma unroll
    for (int i = 0; i < 3; ++i) {
      const int a = 3 * g + i;
      lds[L_QP + wave * DH + (a >> 3) * 64 + c * 8 + (a & 7)] = o3[i];
    }
    if (lane == 0) { lds[L_ML + wave * 2] = M; lds[L_ML + wave * 2 + 1] = l; }
  }
#pragma unroll
  for (int i = 0; i < 5; ++i) wsl[i] = *reinterpret_cast<const u32x4*>(&lds[L_W + (i * NT + tid) * 4]);
  __syncthreads();
  const size_t p1h = WS_P1 + (size_t)(h * NSPLIT) * P1_STRIDE * 8;
  if (wave < 4) {
    if (tid < 194) {
      float M = -INFINITY;
#pragma unroll
      for (int w = 0; w < NW; ++w) M = fmaxf(M, lds[L_ML + 2 * w]);
      float o = 0.f, L = 0.f;
      const int t = tid < DH ? tid : 0;
#pragma unroll
      for (int w = 0; w < NW; ++w) {
        const float f = __builtin_amdgcn_exp2f(lds[L_ML + 2 * w] - M);
        o = fmaf(f, lds[L_QP + w * DH + t], o);
        L = fmaf(f, lds[L_ML + 2 * w + 1], L);
      }
      const float v = tid < DH ? o : (tid == DH ? M : L);
      st_gran(ws, p1h + ((size_t)s * P1_STRIDE + tid) * 8, tag, v);
    }
    const float gate = lds[L_G], gate1 = lds[L_G + 1];
    if (wave < 3) {
      unsigned long long ga[8], gm = 0;
      bool ok[8], okm = false;
#pragma unroll
      for (int i = 0; i < 8; ++i) { ok[i] = false; ga[i] = 0; }
      for (unsigned spin = 0; spin < SPIN_CAP; ++spin) {
#pragma unroll
        for (int i = 0; i < 8; ++i)
          if (!ok[i]) ga[i] = ld_gran(ws, p1h + ((size_t)i * P1_STRIDE + tid) * 8);
        if (!okm) gm = ld_gran(ws, p1h + ((size_t)(lane & 7) * P1_STRIDE + DH + ((lane >> 3) & 1)) * 8);
        bool all = true;
#pragma unroll
        for (int i = 0; i < 8; ++i) { ok[i] = __all((unsigned)(ga[i] >> 32) == tag); all = all && ok[i]; }
        okm = __all((unsigned)(gm >> 32) == tag);
        if (all && okm) break;
        __builtin_amdgcn_s_sleep(1);
      }
      const float val = __uint_as_float((unsigned)gm);
      const float other = dpp<ROR8>(val);
      const float ms = (lane & 8) ? other : val, ls = (lane & 8) ? val : other;
      float M = fmaxf(ms, dpp<XOR1>(ms)); M = fmaxf(M, dpp<XOR2>(M)); M = fmaxf(M, dpp<HMIR>(M));
      const float wgt = __builtin_amdgcn_exp2f(ms - M);
      float L = wgt * ls; L += dpp<XOR1>(L); L += dpp<XOR2>(L); L += dpp<HMIR>(L);
      const float wn = wgt / L;
      float o = 0.f;
#pragma unroll
      for (int i = 0; i < 8; ++i)
        o = fmaf(__builtin_bit_cast(float, __builtin_amdgcn_readlane(__builtin_bit_cast(int, wn), i)),
                 __uint_as_float((unsigned)ga[i]), o);
      lds[L_Z + tid] = gate * bf_round(o);
    } else {
      const int t = lane < FPH ? lane : 0;
      unsigned long long gh[16];
#pragma unroll 1
      for (unsigned spin = 0; spin < SPIN_CAP; ++spin) {
#pragma unroll
        for (int i = 0; i < 8; ++i) {
          gh[2 * i] = ld_gran(ws, pqh + ((size_t)i * NPC + DH + t) * 8);
          gh[2 * i + 1] = ld_gran(ws, pqh + ((size_t)i * NPC + DH + FPH + t) * 8);
        }
        bool ok = true;
#pragma unroll
        for (int i = 0; i < 16; ++i) ok = ok & ((gh[i] >> 24) == tag40);
        if (__all(ok)) break;
        __builtin_amdgcn_s_sleep(1);
      }
      float gs = 0.f, us = 0.f;
#pragma unroll
      for (int i = 0; i < 8; ++i) {
        gs += __uint_as_float(((unsigned)gh[2 * i]) << 8);
        us += __uint_as_float(((unsigned)gh[2 * i + 1]) << 8);
      }
      const float hh = bf_round(bf_round(gs) * bf_round(us));
      if (lane < FPH) lds[L_Z + DH + lane] = gate1 * hh;
    }
  }
  __syncthreads();
  if (tid < 480) {
    const int rg = tid / 10, ch = tid - rg * 10;
    float y[8];
#pragma unroll
    for (int e = 0; e < 8; ++e) y[e] = 0.f;
#pragma unroll
    for (int i = 0; i < 5; ++i) {
      const float z = lds[L_Z + rg * 5 + i];
#pragma unroll
      for (int k = 0; k < 4; ++k) {
        y[2 * k] = fmaf(z, bf_lo(wsl[i][k]), y[2 * k]);
        y[2 * k + 1] = fmaf(z, bf_hi(wsl[i][k]), y[2 * k + 1]);
      }
    }
    *reinterpret_cast<f32x4*>(&lds[L_PART + rg * 88 + ch * 8]) = f32x4{y[0], y[1], y[2], y[3]};
    *reinterpret_cast<f32x4*>(&lds[L_PART + rg * 88 + ch * 8 + 4]) = f32x4{y[4], y[5], y[6], y[7]};
  }
  __syncthreads();
  if (tid < 320) {
    const int col = tid >> 2, qt = tid & 3;
    float y = 0.f;
#pragma unroll
    for (int k = 0; k < 12; ++k) y += lds[L_PART + (4 * k + qt) * 88 + col];
    y += dpp<XOR1>(y); y += dpp<XOR2>(y);
    if (qt == 0) st_gran(ws, WS_P2 + ((size_t)(s * HQ + h) * CPW + col) * 8, tag, y);
  }
  if (wave == 7 && h < 20) {
    const int hh = lane >> 1, cp = (lane & 1) * 2;
    const size_t base = WS_P2 + ((size_t)(s * HQ + hh) * CPW + h * 4 + cp) * 8;
    unsigned long long g0 = 0, g1 = 0;
    for (unsigned spin = 0; spin < SPIN_CAP; ++spin) {
      g0 = ld_gran(ws, base); g1 = ld_gran(ws, base + 8);
      if (__all((unsigned)(g0 >> 32) == tag && (unsigned)(g1 >> 32) == tag)) break;
      __builtin_amdgcn_s_sleep(1);
    }
    float y0 = __uint_as_float((unsigned)g0), y1 = __uint_as_float((unsigned)g1);
    y0 += dpp<XOR2>(y0); y1 += dpp<XOR2>(y1);
    y0 += dpp<XOR1>(dpp<HMIR>(y0)); y1 += dpp<XOR1>(dpp<HMIR>(y1));
    y0 += dpp<ROR8>(y0); y1 += dpp<ROR8>(y1);
    y0 = swap16_sum(y0, y0); y1 = swap16_sum(y1, y1);
    y0 = swap32_sum(y0, y0); y1 = swap32_sum(y1, y1);
    if (lane < 2) *reinterpret_cast<unsigned*>(out + s * CPW + h * 4 + cp) = bf_pack(y0, y1);
    if (lane == 0 && h == 0 && s == 0) __hip_atomic_store((gu64*)(ws + WS_EPOCH), ep, RLX_AGENT);
  }
}

}

extern "C" void kernel_launch(void* const* d_in, const int* in_sizes, int n_in,
                              void* d_out, int out_size, void* d_ws, size_t ws_size,
                              hipStream_t stream) {
  (void)in_sizes; (void)n_in; (void)out_size; (void)ws_size;
  fused_decode<<<dim3(NWG), dim3(NT), 0, stream>>>(
      (const unsigned short*)d_in[0], (const unsigned short*)d_in[1], (const unsigned short*)d_in[2],
      (const unsigned short*)d_in[3], (const unsigned short*)d_in[4], (const unsigned short*)d_in[6],
      (const unsigned short*)d_in[7], (const unsigned short*)d_in[8], (const unsigned short*)d_in[9],
      (unsigned short*)d_out, (unsigned char*)d_ws);
}
```

```cpp
#include <hip/hip_runtime.h>
#include <hip/hip_bf16.h>
#include <stdint.h>

#define RING 4
#define RING_PRO 1
#define NCTL 1
#define KV_AUX 2

namespace {

constexpr int D = 640, HQ = 32, SK = 8192, DH = 192, FF = 1536;
constexpr int NSPLIT = 8, NWG = 256, NT = 512, NW = 8;
constexpr int KEYS_WG = SK / NSPLIT;
constexpr int NB = KEYS_WG / 8 / NW;
constexpr int NQ = HQ * DH;
constexpr int CPW = D / NSPLIT;
constexpr int FPH = FF / HQ;

constexpr int NPC = DH + 2 * FPH;

constexpr size_t WS_EPOCH = 0;
constexpr int P1_STRIDE = 208;
constexpr size_t WS_P1 = 4096;
constexpr size_t WS_P2 = WS_P1 + (size_t)NWG * P1_STRIDE * 8;
constexpr size_t WS_PQ = WS_P2 + (size_t)NWG * CPW * 8;
constexpr size_t WS_END = WS_PQ + (size_t)NWG * NPC * 8;

constexpr int L_W = 0;
constexpr int L_QP = L_W + 5 * NT * 4;
constexpr int L_FP = L_QP + NW * DH;
constexpr int L_ML = L_FP + NW * 16;
constexpr int L_Q = L_ML + 16;
constexpr int L_G = L_Q + 96;
constexpr int L_Z = L_G + 4;
constexpr int L_PART = L_Z + 240;
constexpr int L_END = L_PART + 15 * NPC;

typedef unsigned u32x4 __attribute__((ext_vector_type(4)));
typedef float f32x4 __attribute__((ext_vector_type(4)));
typedef __bf16 bf16x2 __attribute__((ext_vector_type(2)));
typedef __attribute__((address_space(1))) unsigned long long gu64;
typedef __attribute__((address_space(1))) unsigned gu32;
#define RLX_AGENT __ATOMIC_RELAXED, __HIP_MEMORY_SCOPE_AGENT

__device__ __forceinline__ float bf_lo(unsigned w) { return __builtin_bit_cast(float, w << 16); }
__device__ __forceinline__ float bf_hi(unsigned w) { return __builtin_bit_cast(float, w & 0xffff0000u); }
__device__ __forceinline__ float bf_round(float v) { return (float)(__bf16)v; }
__device__ __forceinline__ unsigned bf_pack(float lo, float hi) {
  bf16x2 p; p[0] = (__bf16)lo; p[1] = (__bf16)hi; return __builtin_bit_cast(unsigned, p);
}
__device__ __forceinline__ float dot2(unsigned a, unsigned b, float c) {
  return __builtin_amdgcn_fdot2_f32_bf16(__builtin_bit_cast(bf16x2, a), __builtin_bit_cast(bf16x2, b), c, false);
}
template <int CTRL> __device__ __forceinline__ float dpp(float x) {
  return __builtin_bit_cast(float, __builtin_amdgcn_mov_dpp(__builtin_bit_cast(int, x), CTRL, 0xf, 0xf, true));
}
constexpr int XOR1 = 0xB1, XOR2 = 0x4E, HMIR = 0x141, ROR8 = 0x128, ROR4 = 0x124;

__device__ __forceinline__ float swap32_sum(float a, float b) {
  auto r = __builtin_amdgcn_permlane32_swap(__float_as_uint(a), __float_as_uint(b), false, false);
  return __uint_as_float(r[0]) + __uint_as_float(r[1]);
}
__device__ __forceinline__ float swap16_sum(float a, float b) {
  auto r = __builtin_amdgcn_permlane16_swap(__float_as_uint(a), __float_as_uint(b), false, false);
  return __uint_as_float(r[0]) + __uint_as_float(r[1]);
}
__device__ __forceinline__ float swap32_max(float a) {
  auto r = __builtin_amdgcn_permlane32_swap(__float_as_uint(a), __float_as_uint(a), false, false);
  return fmaxf(__uint_as_float(r[0]), __uint_as_float(r[1]));
}
__device__ __forceinline__ float swap16_max(float a) {
  auto r = __builtin_amdgcn_permlane16_swap(__float_as_uint(a), __float_as_uint(a), false, false);
  return fmaxf(__uint_as_float(r[0]), __uint_as_float(r[1]));
}
__device__ __forceinline__ void reduce24(const float (&v)[24], float (&o)[3], int g) {
  float a[12], b[6];
#pragma unroll
  for (int i = 0; i < 12; ++i) a[i] = swap32_sum(v[i], v[i + 12]);
#pragma unroll
  for (int i = 0; i < 6; ++i) b[i] = swap16_sum(a[i], a[i + 6]);
#pragma unroll
  for (int i = 0; i < 3; ++i) {
    const float lo = b[i] + dpp<ROR8>(b[i]), hi = b[i + 3] + dpp<ROR8>(b[i + 3]);
    o[i] = (g & 1) ? hi : lo;
  }
}
__device__ __forceinline__ void st_gran(unsigned char* ws, size_t off, unsigned tag, float v) {
  __hip_atomic_store((gu64*)(ws + off), ((unsigned long long)tag << 32) | __float_as_uint(v), RLX_AGENT);
}
__device__ __forceinline__ unsigned long long ld_gran(unsigned char* ws, size_t off) {
  return __hip_atomic_load((gu64*)(ws + off), RLX_AGENT);
}
__device__ __forceinline__ __amdgpu_buffer_rsrc_t mk_rsrc(const void* p, unsigned bytes) {
  return __builtin_amdgcn_make_buffer_rsrc((void*)p, 0, bytes, 0x00020000);
}
__device__ __forceinline__ u32x4 ldw(const unsigned short* p) {
  return __builtin_nontemporal_load(reinterpret_cast<const u32x4*>(p));
}
constexpr unsigned SPIN_CAP = 1u << 18;

__global__ void __launch_bounds__(NT, 2)
fused_decode(const unsigned short* __restrict__ x, const unsigned short* __restrict__ kc,
             const unsigned short* __restrict__ vc, const unsigned short* __restrict__ wq,
             const unsigned short* __restrict__ wo, const unsigned short* __restrict__ wg,
             const unsigned short* __restrict__ wu, const unsigned short* __restrict__ wd,
             const unsigned short* __restrict__ gw, unsigned short* __restrict__ out,
             unsigned char* ws) {
  __shared__ __attribute__((aligned(16))) float lds[L_END];
  const int bid = blockIdx.x;
  const int h = (bid & 7) * 4 + ((bid >> 3) & 3), s = bid >> 5;
  const int tid = threadIdx.x, lane = tid & 63, wave = __builtin_amdgcn_readfirstlane(tid >> 6);
  const int g = lane >> 3, c = lane & 7;

  unsigned long long ep = __hip_atomic_load((gu64*)(ws + WS_EPOCH), RLX_AGENT);

  const int prg = tid / 36, pch = tid - prg * 36;
  u32x4 wqv[6];
  unsigned short xq[6];
  {
    const unsigned short* pb = pch < 24 ? wq + h * DH + pch * 8
                             : (pch < 30 ? wg + h * FPH + (pch - 24) * 8 : wu + h * FPH + (pch - 30) * 8);
    const int ld = pch < 24 ? NQ : FF;
#pragma unroll
    for (int i = 0; i < 6; ++i) {
      const int r = prg + 14 * i;
      const int row = s * 80 + (r < 80 ? r : 0);
      wqv[i] = ldw(pb + (size_t)row * ld);
      xq[i] = x[row];
    }
  }
  __builtin_amdgcn_sched_barrier(0);
  __builtin_amdgcn_s_barrier();
  __builtin_amdgcn_sched_barrier(0);

  const size_t kvbase = (size_t)h * SK * DH;
  const __amdgpu_buffer_rsrc_t rk = mk_rsrc(kc + kvbase, SK * DH * 2);
  const __amdgpu_buffer_rsrc_t rv = mk_rsrc(vc + kvbase, SK * DH * 2);
  const int kvoff = s * 24576 + wave * 3072 + g * 384 + c * 16;
#define BSTR (8 * 24576)
  u32x4 kb[RING][3], vb[RING][3];
#define ISSUE_AT(vo_, b_)                                                                                  \
  do {                                                                                                     \
    _Pragma("unroll") for (int j_ = 0; j_ < 3; ++j_)                                                       \
        kb[(b_) % RING][j_] = __builtin_amdgcn_raw_buffer_load_b128(rk, (vo_) + j_ * 128, (b_) * BSTR, KV_AUX); \
    _Pragma("unroll") for (int j_ = 0; j_ < 3; ++j_)                                                       \
        vb[(b_) % RING][j_] = __builtin_amdgcn_raw_buffer_load_b128(rv, (vo_) + j_ * 128, (b_) * BSTR, KV_AUX); \
    __builtin_amdgcn_sched_barrier(0);                                                                     \
  } while (0)
#define ISSUE(b_) ISSUE_AT(kvoff, b_)
#pragma unroll
  for (int b = 0; b < RING_PRO; ++b) ISSUE(b);
  __builtin_amdgcn_sched_barrier(0);
  {
    float qa[8];
#pragma unroll
    for (int e = 0; e < 8; ++e) qa[e] = 0.f;
#pragma unroll
    for (int i = 0; i < 6; ++i) {
      const float xv = (prg + 14 * i < 80) ? bf_lo((unsigned)xq[i]) : 0.f;
#pragma unroll
      for (int k = 0; k < 4; ++k) {
        qa[2 * k] = fmaf(xv, bf_lo(wqv[i][k]), qa[2 * k]);
        qa[2 * k + 1] = fmaf(xv, bf_hi(wqv[i][k]), qa[2 * k + 1]);
      }
    }
    *reinterpret_cast<f32x4*>(&lds[L_PART + prg * NPC + pch * 8]) = f32x4{qa[0], qa[1], qa[2], qa[3]};
    *reinterpret_cast<f32x4*>(&lds[L_PART + prg * NPC + pch * 8 + 4]) = f32x4{qa[4], qa[5], qa[6], qa[7]};
  }
  __syncthreads();
  asm volatile("" : "+v"(ep));
  ep += 1ull;
  const unsigned tag = (unsigned)ep;
  const unsigned long long tag40 = ep & 0xffffffffffull;
  const size_t pqh = WS_PQ + (size_t)(h * NSPLIT) * NPC * 8;
  if (tid < NPC) {
    float v = 0.f;
#pragma unroll
    for (int r = 0; r < 14; ++r) v += lds[L_PART + r * NPC + tid];
    const unsigned vb24 = (__float_as_uint(v) + 0x80u) >> 8;
    __hip_atomic_store((gu64*)(ws + pqh + ((size_t)s * NPC + tid) * 8), (tag40 << 24) | vb24, RLX_AGENT);
  }
  if (tid < 194) st_gran(ws, WS_P1 + ((size_t)(h * NSPLIT + s) * P1_STRIDE + tid) * 8, ~tag, 0.f);
  if (tid < 320 && (tid & 3) == 0) st_gran(ws, WS_P2 + ((size_t)(s * HQ + h) * CPW + (tid >> 2)) * 8, ~tag, 0.f);
  __builtin_amdgcn_sched_barrier(0);
  u32x4 wsl[5];
  {
    const int rg = tid / 10, ch = tid - rg * 10;
#pragma unroll
    for (int i = 0; i < 5; ++i) {
      const int r = (tid < 480) ? rg * 5 + i : 0;
      const unsigned short* p = (r < DH) ? wo + (size_t)(h * DH + r) * D : wd + (size_t)(h * FPH + (r - DH)) * D;
      wsl[i] = ldw(p + s * CPW + ch * 8);
    }
  }
  unsigned gx[5], gg[5];
  if (wave == 7) {
#pragma unroll
    for (int i = 0; i < 5; ++i) {
      gx[i] = *reinterpret_cast<const unsigned*>(x + (i * 64 + lane) * 2);
      gg[i] = *reinterpret_cast<const unsigned*>(gw + (i * 64 + lane) * 2);
    }
  }
  __builtin_amdgcn_sched_barrier(0);
  {
    const int vo = wave < NCTL ? kvoff + (1 << 24) : kvoff;
#pragma unroll
    for (int b = RING_PRO; b < RING; ++b) ISSUE_AT(vo, b);
  }
  __builtin_amdgcn_sched_barrier(0);

#pragma unroll
  for (int i = 0; i < 5; ++i) *reinterpret_cast<u32x4*>(&lds[L_W + (i * NT + tid) * 4]) = wsl[i];
  if (wave == 7) {
    float acc = 0.f;
#pragma unroll
    for (int i = 0; i < 5; ++i) {
      acc += bf_round(bf_lo(gx[i]) * bf_lo(gg[i]));
      acc += bf_round(bf_hi(gx[i]) * bf_hi(gg[i]));
    }
    acc += dpp<XOR1>(acc); acc += dpp<XOR2>(acc); acc += dpp<HMIR>(acc); acc += dpp<ROR8>(acc);
    acc = swap16_sum(acc, acc); acc = swap32_sum(acc, acc);
    const float z = bf_round(acc);
    const float sg = bf_round(1.f / (1.f + __expf(-z)));
    if (lane == 0) { lds[L_G] = sg; lds[L_G + 1] = bf_round(1.f - sg); }
  }
  if (wave < NCTL) {
    constexpr int NG = 3 / NCTL;
    unsigned long long gq[NG][8];
#pragma unroll 1
    for (unsigned spin = 0; spin < SPIN_CAP; ++spin) {
#pragma unroll
      for (int k = 0; k < NG; ++k)
#pragma unroll
        for (int i = 0; i < 8; ++i) gq[k][i] = ld_gran(ws, pqh + ((size_t)i * NPC + k * 64 + tid) * 8);
      bool ok = true;
#pragma unroll
      for (int k = 0; k < NG; ++k)
#pragma unroll
        for (int i = 0; i < 8; ++i) ok = ok & ((gq[k][i] >> 24) == tag40);
      if (__all(ok)) break;
    }
#pragma unroll
    for (int k = 0; k < NG; ++k) {
      float q = 0.f;
#pragma unroll
      for (int i = 0; i < 8; ++i) q += __uint_as_float(((unsigned)gq[k][i]) << 8);
      const float qn = dpp<XOR1>(q);
      if ((tid & 1) == 0) reinterpret_cast<unsigned*>(lds)[L_Q + ((k * 64 + tid) >> 1)] = bf_pack(q, qn);
    }
    __syncthreads();
#pragma unroll
    for (int b = RING_PRO; b < RING; ++b) ISSUE(b);
  } else {
    __syncthreads();
  }
  __builtin_amdgcn_sched_barrier(0);
  u32x4 qv[3];
#pragma unroll
  for (int j = 0; j < 3; ++j) qv[j] = *reinterpret_cast<const u32x4*>(&lds[L_Q + (c + 8 * j) * 4]);

  constexpr float C2 = 0.07216878364870322f * 1.4426950408889634f;
  float m = -INFINITY, l = 0.f, acc[24];
#pragma unroll
  for (int k = 0; k < 24; ++k) acc[k] = 0.f;
#pragma unroll
  for (int b = 0; b < NB; ++b) {
    const int sl = b % RING;
    float d0 = 0.f, d1 = 0.f, d2 = 0.f;
#pragma unroll
    for (int k = 0; k < 4; ++k) {
      d0 = dot2(kb[sl][0][k], qv[0][k], d0);
      d1 = dot2(kb[sl][1][k], qv[1][k], d1);
      d2 = dot2(kb[sl][2][k], qv[2][k], d2);
    }
    float sc = d0 + d1 + d2;
    sc += dpp<XOR1>(sc); sc += dpp<XOR2>(sc); sc += dpp<HMIR>(sc);
    sc *= C2;
    const float mn = fmaxf(m, sc);
    const float alpha = __builtin_amdgcn_exp2f(m - mn), p = __builtin_amdgcn_exp2f(sc - mn);
    m = mn;
    l = fmaf(l, alpha, p);
#pragma unroll
    for (int j = 0; j < 3; ++j)
#pragma unroll
      for (int k = 0; k < 4; ++k) {
        acc[j * 8 + 2 * k] = fmaf(p, bf_lo(vb[sl][j][k]), acc[j * 8 + 2 * k] * alpha);
        acc[j * 8 + 2 * k + 1] = fmaf(p, bf_hi(vb[sl][j][k]), acc[j * 8 + 2 * k + 1] * alpha);
      }
#pragma unroll
    for (int k = 0; k < 24; k += 4) asm volatile("" : "+v"(acc[k]), "+v"(acc[k + 1]), "+v"(acc[k + 2]), "+v"(acc[k + 3]));
    __builtin_amdgcn_sched_barrier(0);
    if (b + RING < NB) ISSUE(b + RING);
    __builtin_amdgcn_sched_barrier(0);
  }
#undef ISSUE
#undef ISSUE_AT
#undef BSTR

  {
    float M = fmaxf(m, dpp<ROR8>(m));
    M = swap16_max(M); M = swap32_max(M);
    const float f = __builtin_amdgcn_exp2f(m - M);
    l *= f;
#pragma unroll
    for (int k = 0; k < 24; ++k) acc[k] *= f;
    float o3[3];
    reduce24(acc, o3, g);
    l += dpp<ROR8>(l); l = swap16_sum(l, l); l = swap32_sum(l, l);
#pragma unroll
    for (int i = 0; i < 3; ++i) {
      const int a = 3 * g + i;
      lds[L_QP + wave * DH + (a >> 3) * 64 + c * 8 + (a & 7)] = o3[i];
    }
    if (lane == 0) { lds[L_ML + wave * 2] = M; lds[L_ML + wave * 2 + 1] = l; }
  }
#pragma unroll
  for (int i = 0; i < 5; ++i) wsl[i] = *reinterpret_cast<const u32x4*>(&lds[L_W + (i * NT + tid) * 4]);
  __syncthreads();
  const size_t p1h = WS_P1 + (size_t)(h * NSPLIT) * P1_STRIDE * 8;
  if (wave < 4) {
    if (tid < 194) {
      float M = -INFINITY;
#pragma unroll
      for (int w = 0; w < NW; ++w) M = fmaxf(M, lds[L_ML + 2 * w]);
      float o = 0.f, L = 0.f;
      const int t = tid < DH ? tid : 0;
#pragma unroll
      for (int w = 0; w < NW; ++w) {
        const float f = __builtin_amdgcn_exp2f(lds[L_ML + 2 * w] - M);
        o = fmaf(f, lds[L_QP + w * DH + t], o);
        L = fmaf(f, lds[L_ML + 2 * w + 1], L);
      }
      const float v = tid < DH ? o : (tid == DH ? M : L);
      st_gran(ws, p1h + ((size_t)s * P1_STRIDE + tid) * 8, tag, v);
    }
    const float gate = lds[L_G], gate1 = lds[L_G + 1];
    if (wave < 3) {
      unsigned long long ga[8], gm = 0;
      bool ok[8], okm = false;
#pragma unroll
      for (int i = 0; i < 8; ++i) { ok[i] = false; ga[i] = 0; }
      for (unsigned spin = 0; spin < SPIN_CAP; ++spin) {
#pragma unroll
        for (int i = 0; i < 8; ++i)
          if (!ok[i]) ga[i] = ld_gran(ws, p1h + ((size_t)i * P1_STRIDE + tid) * 8);
        if (!okm) gm = ld_gran(ws, p1h + ((size_t)(lane & 7) * P1_STRIDE + DH + ((lane >> 3) & 1)) * 8);
        bool all = true;
#pragma unroll
        for (int i = 0; i < 8; ++i) { ok[i] = __all((unsigned)(ga[i] >> 32) == tag); all = all && ok[i]; }
        okm = __all((unsigned)(gm >> 32) == tag);
        if (all && okm) break;
        __builtin_amdgcn_s_sleep(1);
      }
      const float val = __uint_as_float((unsigned)gm);
      const float other = dpp<ROR8>(val);
      const float ms = (lane & 8) ? other : val, ls = (lane & 8) ? val : other;
      float M = fmaxf(ms, dpp<XOR1>(ms)); M = fmaxf(M, dpp<XOR2>(M)); M = fmaxf(M, dpp<HMIR>(M));
      const float wgt = __builtin_amdgcn_exp2f(ms - M);
      float L = wgt * ls; L += dpp<XOR1>(L); L += dpp<XOR2>(L); L += dpp<HMIR>(L);
      const float wn = wgt / L;
      float o = 0.f;
#pragma unroll
      for (int i = 0; i < 8; ++i)
        o = fmaf(__builtin_bit_cast(float, __builtin_amdgcn_readlane(__builtin_bit_cast(int, wn), i)),
                 __uint_as_float((unsigned)ga[i]), o);
      lds[L_Z + tid] = gate * bf_round(o);
    } else {
      const int t = lane < FPH ? lane : 0;
      unsigned long long gh[16];
#pragma unroll 1
      for (unsigned spin = 0; spin < SPIN_CAP; ++spin) {
#pragma unroll
        for (int i = 0; i < 8; ++i) {
          gh[2 * i] = ld_gran(ws, pqh + ((size_t)i * NPC + DH + t) * 8);
          gh[2 * i + 1] = ld_gran(ws, pqh + ((size_t)i * NPC + DH + FPH + t) * 8);
        }
        bool ok = true;
#pragma unroll
        for (int i = 0; i < 16; ++i) ok = ok & ((gh[i] >> 24) == tag40);
        if (__all(ok)) break;
        __builtin_amdgcn_s_sleep(1);
      }
      float gs = 0.f, us = 0.f;
#pragma unroll
      for (int i = 0; i < 8; ++i) {
        gs += __uint_as_float(((unsigned)gh[2 * i]) << 8);
        us += __uint_as_float(((unsigned)gh[2 * i + 1]) << 8);
      }
      const float hh = bf_round(bf_round(gs) * bf_round(us));
      if (lane < FPH) lds[L_Z + DH + lane] = gate1 * hh;
    }
  }
  __syncthreads();
  if (tid < 480) {
    const int rg = tid / 10, ch = tid - rg * 10;
    float y[8];
#pragma unroll
    for (int e = 0; e < 8; ++e) y[e] = 0.f;
#pragma unroll
    for (int i = 0; i < 5; ++i) {
      const float z = lds[L_Z + rg * 5 + i];
#pragma unroll
      for (int k = 0; k < 4; ++k) {
        y[2 * k] = fmaf(z, bf_lo(wsl[i][k]), y[2 * k]);
        y[2 * k + 1] = fmaf(z, bf_hi(wsl[i][k]), y[2 * k + 1]);
      }
    }
    *reinterpret_cast<f32x4*>(&lds[L_PART + rg * 88 + ch * 8]) = f32x4{y[0], y[1], y[2], y[3]};
    *reinterpret_cast<f32x4*>(&lds[L_PART + rg * 88 + ch * 8 + 4]) = f32x4{y[4], y[5], y[6], y[7]};
  }
  __syncthreads();
  if (tid < 320) {
    const int col = tid >> 2, qt = tid & 3;
    float y = 0.f;
#pragma unroll
    for (int k = 0; k < 12; ++k) y += lds[L_PART + (4 * k + qt) * 88 + col];
    y += dpp<XOR1>(y); y += dpp<XOR2>(y);
    if (qt == 0) st_gran(ws, WS_P2 + ((size_t)(s * HQ + h) * CPW + col) * 8, tag, y);
  }
  if (wave == 7 && h < 20) {
    const int hh = lane >> 1, cp = (lane & 1) * 2;
    const size_t base = WS_P2 + ((size_t)(s * HQ + hh) * CPW + h * 4 + cp) * 8;
    unsigned long long g0 = 0, g1 = 0;
    for (unsigned spin = 0; spin < SPIN_CAP; ++spin) {
      g0 = ld_gran(ws, base); g1 = ld_gran(ws, base + 8);
      if (__all((unsigned)(g0 >> 32) == tag && (unsigned)(g1 >> 32) == tag)) break;
      __builtin_amdgcn_s_sleep(1);
    }
    float y0 = __uint_as_float((unsigned)g0), y1 = __uint_as_float((unsigned)g1);
    y0 += dpp<XOR2>(y0); y1 += dpp<XOR2>(y1);
    y0 += dpp<XOR1>(dpp<HMIR>(y0)); y1 += dpp<XOR1>(dpp<HMIR>(y1));
    y0 += dpp<ROR8>(y0); y1 += dpp<ROR8>(y1);
    y0 = swap16_sum(y0, y0); y1 = swap16_sum(y1, y1);
    y0 = swap32_sum(y0, y0); y1 = swap32_sum(y1, y1);
    if (lane < 2) *reinterpret_cast<unsigned*>(out + s * CPW + h * 4 + cp) = bf_pack(y0, y1);
    if (lane == 0 && h == 0 && s == 0) __hip_atomic_store((gu64*)(ws + WS_EPOCH), ep, RLX_AGENT);
  }
}

}

extern "C" void kernel_launch(void* const* d_in, const int* in_sizes, int n_in,
                              void* d_out, int out_size, void* d_ws, size_t ws_size,
                              hipStream_t stream) {
  (void)in_sizes; (void)n_in; (void)out_size; (void)ws_size;
  fused_decode<<<dim3(NWG), dim3(NT), 0, stream>>>(
      (const unsigned short*)d_in[0], (const unsigned short*)d_in[1], (const unsigned short*)d_in[2],
      (const unsigned short*)d_in[3], (const unsigned short*)d_in[4], (const unsigned short*)d_in[6],
      (const unsigned short*)d_in[7], (const unsigned short*)d_in[8], (const unsigned short*)d_in[9],
      (unsigned short*)d_out, (unsigned char*)d_ws);
}
```

```cpp
#include <hip/hip_runtime.h>
#include <hip/hip_bf16.h>
#include <stdint.h>

#define RING 4
#define RING_PRO 2
#define NCTL 1
#define KV_AUX 2

namespace {

constexpr int D = 640, HQ = 32, SK = 8192, DH = 192, FF = 1536;
constexpr int NSPLIT = 8, NWG = 256, NT = 512, NW = 8;
constexpr int KEYS_WG = SK / NSPLIT;
constexpr int NB = KEYS_WG / 8 / NW;
constexpr int NQ = HQ * DH;
constexpr int CPW = D / NSPLIT;
constexpr int FPH = FF / HQ;

constexpr int NPC = DH + 2 * FPH;

constexpr size_t WS_EPOCH = 0;
constexpr int P1_STRIDE = 208;
constexpr size_t WS_P1 = 4096;
constexpr size_t WS_P2 = WS_P1 + (size_t)NWG * P1_STRIDE * 8;
constexpr size_t WS_PQ = WS_P2 + (size_t)NWG * CPW * 8;
constexpr size_t WS_END = WS_PQ + (size_t)NWG * NPC * 8;

constexpr int WPITCH = 160;
constexpr int L_W = 0;
constexpr int L_QP = L_W + 256 * WPITCH / 4;
constexpr int L_ML = L_QP + NW * DH;
constexpr int L_Q = L_ML + 16;
constexpr int L_G = L_Q + 96;
constexpr int L_H = L_G + 4;
constexpr int L_Z = L_H + 32;
constexpr int L_PART = L_Z + 96;
constexpr int L_END = L_PART + 15 * NPC;

typedef unsigned u32x4 __attribute__((ext_vector_type(4)));
typedef float f32x4 __attribute__((ext_vector_type(4)));
typedef float f32x2 __attribute__((ext_vector_type(2)));
typedef __bf16 bf16x2 __attribute__((ext_vector_type(2)));
typedef __bf16 bf16x8 __attribute__((ext_vector_type(8)));
typedef short i16x4 __attribute__((ext_vector_type(4)));
typedef unsigned u32x2 __attribute__((ext_vector_type(2)));
typedef __attribute__((address_space(3))) i16x4 lds_i16x4;
typedef __attribute__((address_space(1))) unsigned long long gu64;
typedef __attribute__((address_space(1))) unsigned gu32;
#define RLX_AGENT __ATOMIC_RELAXED, __HIP_MEMORY_SCOPE_AGENT

__device__ __forceinline__ float bf_lo(unsigned w) { return __builtin_bit_cast(float, w << 16); }
__device__ __forceinline__ float bf_hi(unsigned w) { return __builtin_bit_cast(float, w & 0xffff0000u); }
__device__ __forceinline__ float bf_round(float v) { return (float)(__bf16)v; }
__device__ __forceinline__ unsigned bf_pack(float lo, float hi) {
  bf16x2 p; p[0] = (__bf16)lo; p[1] = (__bf16)hi; return __builtin_bit_cast(unsigned, p);
}
__device__ __forceinline__ float dot2(unsigned a, unsigned b, float c) {
  return __builtin_amdgcn_fdot2_f32_bf16(__builtin_bit_cast(bf16x2, a), __builtin_bit_cast(bf16x2, b), c, false);
}
template <int CTRL> __device__ __forceinline__ float dpp(float x) {
  return __builtin_bit_cast(float, __builtin_amdgcn_mov_dpp(__builtin_bit_cast(int, x), CTRL, 0xf, 0xf, true));
}
constexpr int XOR1 = 0xB1, XOR2 = 0x4E, HMIR = 0x141, ROR8 = 0x128, ROR4 = 0x124;

__device__ __forceinline__ float swap32_sum(float a, float b) {
  auto r = __builtin_amdgcn_permlane32_swap(__float_as_uint(a), __float_as_uint(b), false, false);
  return __uint_as_float(r[0]) + __uint_as_float(r[1]);
}
__device__ __forceinline__ float swap16_sum(float a, float b) {
  auto r = __builtin_amdgcn_permlane16_swap(__float_as_uint(a), __float_as_uint(b), false, false);
  return __uint_as_float(r[0]) + __uint_as_float(r[1]);
}
__device__ __forceinline__ float swap32_max(float a) {
  auto r = __builtin_amdgcn_permlane32_swap(__float_as_uint(a), __float_as_uint(a), false, false);
  return fmaxf(__uint_as_float(r[0]), __uint_as_float(r[1]));
}
__device__ __forceinline__ float swap16_max(float a) {
  auto r = __builtin_amdgcn_permlane16_swap(__float_as_uint(a), __float_as_uint(a), false, false);
  return fmaxf(__uint_as_float(r[0]), __uint_as_float(r[1]));
}
__device__ __forceinline__ void reduce24(const float (&v)[24], float (&o)[3], int g) {
  float a[12], b[6];
#pragma unroll
  for (int i = 0; i < 12; ++i) a[i] = swap32_sum(v[i], v[i + 12]);
#pragma unroll
  for (int i = 0; i < 6; ++i) b[i] = swap16_sum(a[i], a[i + 6]);
#pragma unroll
  for (int i = 0; i < 3; ++i) {
    const float lo = b[i] + dpp<ROR8>(b[i]), hi = b[i + 3] + dpp<ROR8>(b[i + 3]);
    o[i] = (g & 1) ? hi : lo;
  }
}
__device__ __forceinline__ void st_gran(unsigned char* ws, size_t off, unsigned tag, float v) {
  __hip_atomic_store((gu64*)(ws + off), ((unsigned long long)tag << 32) | __float_as_uint(v), RLX_AGENT);
}
__device__ __forceinline__ unsigned long long ld_gran(unsigned char* ws, size_t off) {
  return __hip_atomic_load((gu64*)(ws + off), RLX_AGENT);
}
__device__ __forceinline__ __amdgpu_buffer_rsrc_t mk_rsrc(const void* p, unsigned bytes) {
  return __builtin_amdgcn_make_buffer_rsrc((void*)p, 0, bytes, 0x00020000);
}
__device__ __forceinline__ u32x4 ldw(const unsigned short* p) {
  return __builtin_nontemporal_load(reinterpret_cast<const u32x4*>(p));
}
constexpr unsigned SPIN_CAP = 1u << 18;

__global__ void __launch_bounds__(NT, 2)
fused_decode(const unsigned short* __restrict__ x, const unsigned short* __restrict__ kc,
             const unsigned short* __restrict__ vc, const unsigned short* __restrict__ wq,
             const unsigned short* __restrict__ wo, const unsigned short* __restrict__ wg,
             const unsigned short* __restrict__ wu, const unsigned short* __restrict__ wd,
             const unsigned short* __restrict__ gw, unsigned short* __restrict__ out,
             unsigned char* ws) {
  __shared__ __attribute__((aligned(16))) float lds[L_END];
  const int bid = blockIdx.x;
  const int h = (bid & 7) * 4 + ((bid >> 3) & 3), s = bid >> 5;
  const int tid = threadIdx.x, lane = tid & 63, wave = __builtin_amdgcn_readfirstlane(tid >> 6);
  const int g = lane >> 3, c = lane & 7;

  unsigned long long ep = __hip_atomic_load((gu64*)(ws + WS_EPOCH), RLX_AGENT);

  const int prg = tid / 36, pch = tid - prg * 36;
  u32x4 wqv[6];
  unsigned short xq[6];
  {
    const unsigned short* pb = pch < 24 ? wq + h * DH + pch * 8
                             : (pch < 30 ? wg + h * FPH + (pch - 24) * 8 : wu + h * FPH + (pch - 30) * 8);
    const int ld = pch < 24 ? NQ : FF;
#pragma unroll
    for (int i = 0; i < 6; ++i) {
      const int r = prg + 14 * i;
      const int row = s * 80 + (r < 80 ? r : 0);
      wqv[i] = ldw(pb + (size_t)row * ld);
      xq[i] = x[row];
    }
  }
  __builtin_amdgcn_sched_barrier(0);
  __builtin_amdgcn_s_barrier();
  __builtin_amdgcn_sched_barrier(0);

  const size_t kvbase = (size_t)h * SK * DH;
  const __amdgpu_buffer_rsrc_t rk = mk_rsrc(kc + kvbase, SK * DH * 2);
  const __amdgpu_buffer_rsrc_t rv = mk_rsrc(vc + kvbase, SK * DH * 2);
  const int kvoff = s * 24576 + wave * 3072 + g * 384 + c * 16;
#define BSTR (8 * 24576)
  u32x4 kb[RING][3], vb[RING][3];
#define ISSUE_AT(vo_, b_)                                                                                  \
  do {                                                                                                     \
    _Pragma("unroll") for (int j_ = 0; j_ < 3; ++j_)                                                       \
        kb[(b_) % RING][j_] = __builtin_amdgcn_raw_buffer_load_b128(rk, (vo_) + j_ * 128, (b_) * BSTR, KV_AUX); \
    _Pragma("unroll") for (int j_ = 0; j_ < 3; ++j_)                                                       \
        vb[(b_) % RING][j_] = __builtin_amdgcn_raw_buffer_load_b128(rv, (vo_) + j_ * 128, (b_) * BSTR, KV_AUX); \
    __builtin_amdgcn_sched_barrier(0);                                                                     \
  } while (0)
#define ISSUE(b_) ISSUE_AT(kvoff, b_)
#pragma unroll
  for (int b = 0; b < RING_PRO; ++b) ISSUE(b);
  __builtin_amdgcn_sched_barrier(0);
  {
    float qa[8];
#pragma unroll
    for (int e = 0; e < 8; ++e) qa[e] = 0.f;
#pragma unroll
    for (int i = 0; i < 6; ++i) {
      const float xv = (prg + 14 * i < 80) ? bf_lo((unsigned)xq[i]) : 0.f;
#pragma unroll
      for (int k = 0; k < 4; ++k) {
        qa[2 * k] = fmaf(xv, bf_lo(wqv[i][k]), qa[2 * k]);
        qa[2 * k + 1] = fmaf(xv, bf_hi(wqv[i][k]), qa[2 * k + 1]);
      }
    }
    *reinterpret_cast<f32x4*>(&lds[L_PART + prg * NPC + pch * 8]) = f32x4{qa[0], qa[1], qa[2], qa[3]};
    *reinterpret_cast<f32x4*>(&lds[L_PART + prg * NPC + pch * 8 + 4]) = f32x4{qa[4], qa[5], qa[6], qa[7]};
  }
  __syncthreads();
  asm volatile("" : "+v"(ep));
  ep += 1ull;
  const unsigned tag = (unsigned)ep;
  const unsigned long long tag40 = ep & 0xffffffffffull;
  const size_t pqh = WS_PQ + (size_t)(h * NSPLIT) * NPC * 8;
  if (tid < NPC) {
    float v = 0.f;
#pragma unroll
    for (int r = 0; r < 14; ++r) v += lds[L_PART + r * NPC + tid];
    const unsigned vb24 = (__float_as_uint(v) + 0x80u) >> 8;
    __hip_atomic_store((gu64*)(ws + pqh + ((size_t)s * NPC + tid) * 8), (tag40 << 24) | vb24, RLX_AGENT);
  }
  if (tid < 194) st_gran(ws, WS_P1 + ((size_t)(h * NSPLIT + s) * P1_STRIDE + tid) * 8, ~tag, 0.f);
  if (tid < 320 && (tid & 3) == 0) st_gran(ws, WS_P2 + ((size_t)(s * HQ + h) * CPW + (tid >> 2)) * 8, ~tag, 0.f);
  __builtin_amdgcn_sched_barrier(0);
  u32x4 wsl[5];
  {
    const int rg = tid / 10, ch = tid - rg * 10;
#pragma unroll
    for (int i = 0; i < 5; ++i) {
      const int r = (tid < 480) ? rg * 5 + i : 0;
      const unsigned short* p = (r < DH) ? wo + (size_t)(h * DH + r) * D : wd + (size_t)(h * FPH + (r - DH)) * D;
      wsl[i] = ldw(p + s * CPW + ch * 8);
    }
  }
  unsigned gx[5], gg[5];
  if (wave == 7) {
#pragma unroll
    for (int i = 0; i < 5; ++i) {
      gx[i] = *reinterpret_cast<const unsigned*>(x + (i * 64 + lane) * 2);
      gg[i] = *reinterpret_cast<const unsigned*>(gw + (i * 64 + lane) * 2);
    }
  }
  __builtin_amdgcn_sched_barrier(0);
  {
    const int vo = wave < NCTL ? kvoff + (1 << 24) : kvoff;
#pragma unroll
    for (int b = RING_PRO; b < RING; ++b) ISSUE_AT(vo, b);
  }
  __builtin_amdgcn_sched_barrier(0);

  {
    const int rg = tid / 10, ch = tid - rg * 10;
    const int wb = (tid < 480) ? (rg * 5 * WPITCH + ch * 16) / 4 : (240 * WPITCH + (tid - 480) * 16) / 4;
#pragma unroll
    for (int i = 0; i < 5; ++i)
      *reinterpret_cast<u32x4*>(&lds[L_W + wb + i * ((tid < 480) ? WPITCH / 4 : 128)]) = wsl[i];
  }
  if (wave == 7) {
    float acc = 0.f;
#pragma unroll
    for (int i = 0; i < 5; ++i) {
      acc += bf_round(bf_lo(gx[i]) * bf_lo(gg[i]));
      acc += bf_round(bf_hi(gx[i]) * bf_hi(gg[i]));
    }
    acc += dpp<XOR1>(acc); acc += dpp<XOR2>(acc); acc += dpp<HMIR>(acc); acc += dpp<ROR8>(acc);
    acc = swap16_sum(acc, acc); acc = swap32_sum(acc, acc);
    const float z = bf_round(acc);
    const float sg = bf_round(1.f / (1.f + __expf(-z)));
    if (lane == 0) { lds[L_G] = sg; lds[L_G + 1] = bf_round(1.f - sg); }
  }
  if (wave == 0) {
    unsigned long long gq[3][8];
#pragma unroll 1
    for (unsigned spin = 0; spin < SPIN_CAP; ++spin) {
#pragma unroll
      for (int k = 0; k < 3; ++k)
#pragma unroll
        for (int i = 0; i < 8; ++i) gq[k][i] = ld_gran(ws, pqh + ((size_t)i * NPC + k * 64 + lane) * 8);
      bool ok = true;
#pragma unroll
      for (int k = 0; k < 3; ++k)
#pragma unroll
        for (int i = 0; i < 8; ++i) ok = ok & ((gq[k][i] >> 24) == tag40);
      if (__all(ok)) break;
    }
#pragma unroll
    for (int k = 0; k < 3; ++k) {
      float q = 0.f;
#pragma unroll
      for (int i = 0; i < 8; ++i) q += __uint_as_float(((unsigned)gq[k][i]) << 8);
      const float qn = dpp<XOR1>(q);
      if ((lane & 1) == 0) reinterpret_cast<unsigned*>(lds)[L_Q + ((k * 64 + lane) >> 1)] = bf_pack(q, qn);
    }
    __syncthreads();
    const size_t pgu = pqh + (size_t)(DH + (lane < FPH ? lane : 0)) * 8;
    unsigned long long gh[16];
#pragma unroll
    for (int i = 0; i < 16; ++i) gh[i] = ld_gran(ws, pgu + ((size_t)(i >> 1) * NPC + (i & 1) * FPH) * 8);
    __builtin_amdgcn_sched_barrier(0);
#pragma unroll
    for (int b = RING_PRO; b < RING; ++b) ISSUE(b);
    float gs = 0.f, us = 0.f;
    bool okh = true;
#pragma unroll
    for (int i = 0; i < 8; ++i) {
      okh = okh & ((gh[2 * i] >> 24) == tag40) & ((gh[2 * i + 1] >> 24) == tag40);
      gs += __uint_as_float(((unsigned)gh[2 * i]) << 8);
      us += __uint_as_float(((unsigned)gh[2 * i + 1]) << 8);
    }
#pragma unroll 1
    for (unsigned spin = 0; spin < SPIN_CAP && !__all(okh); ++spin) {
#pragma unroll
      for (int i = 0; i < 16; ++i) gh[i] = ld_gran(ws, pgu + ((size_t)(i >> 1) * NPC + (i & 1) * FPH) * 8);
      gs = 0.f; us = 0.f; okh = true;
#pragma unroll
      for (int i = 0; i < 8; ++i) {
        okh = okh & ((gh[2 * i] >> 24) == tag40) & ((gh[2 * i + 1] >> 24) == tag40);
        gs += __uint_as_float(((unsigned)gh[2 * i]) << 8);
        us += __uint_as_float(((unsigned)gh[2 * i + 1]) << 8);
      }
    }
    const float hh = lane < FPH ? bf_round(gs) * bf_round(us) : 0.f;
    const float hn = dpp<XOR1>(hh);
    if ((lane & 1) == 0) reinterpret_cast<unsigned*>(lds)[L_H + (lane >> 1)] = bf_pack(hh, hn);
  } else {
    __syncthreads();
  }
  __builtin_amdgcn_sched_barrier(0);
  u32x4 qv[3];
#pragma unroll
  for (int j = 0; j < 3; ++j) qv[j] = *reinterpret_cast<const u32x4*>(&lds[L_Q + (c + 8 * j) * 4]);

  constexpr float C2 = 0.07216878364870322f * 1.4426950408889634f;
  float m = -INFINITY, l = 0.f, acc[24];
#pragma unroll
  for (int k = 0; k < 24; ++k) acc[k] = 0.f;
#pragma unroll
  for (int b = 0; b < NB; ++b) {
    const int sl = b % RING;
    float d0 = 0.f, d1 = 0.f, d2 = 0.f;
#pragma unroll
    for (int k = 0; k < 4; ++k) {
      d0 = dot2(kb[sl][0][k], qv[0][k], d0);
      d1 = dot2(kb[sl][1][k], qv[1][k], d1);
      d2 = dot2(kb[sl][2][k], qv[2][k], d2);
    }
    float sc = d0 + d1 + d2;
    sc += dpp<XOR1>(sc); sc += dpp<XOR2>(sc); sc += dpp<HMIR>(sc);
    sc *= C2;
    if (__any(sc > m)) {
      float mn = fmaxf(sc, dpp<ROR8>(sc));
      mn = swap16_max(mn); mn = swap32_max(mn);
      mn = fmaxf(mn, m);
      const float alpha = __builtin_amdgcn_exp2f(m - mn);
      m = mn;
      l *= alpha;
#pragma unroll
      for (int k = 0; k < 24; ++k) acc[k] *= alpha;
    }
    const float p = __builtin_amdgcn_exp2f(sc - m);
    l += p;
#pragma unroll
    for (int j = 0; j < 3; ++j)
#pragma unroll
      for (int k = 0; k < 4; ++k) {
        f32x2 a2 = {acc[j * 8 + 2 * k], acc[j * 8 + 2 * k + 1]};
        a2 = __builtin_elementwise_fma(f32x2{p, p}, f32x2{bf_lo(vb[sl][j][k]), bf_hi(vb[sl][j][k])}, a2);
        acc[j * 8 + 2 * k] = a2[0]; acc[j * 8 + 2 * k + 1] = a2[1];
      }
#pragma unroll
    for (int k = 0; k < 24; k += 4) asm volatile("" : "+v"(acc[k]), "+v"(acc[k + 1]), "+v"(acc[k + 2]), "+v"(acc[k + 3]));
    __builtin_amdgcn_sched_barrier(0);
    if (b + RING < NB) ISSUE(b + RING);
    __builtin_amdgcn_sched_barrier(0);
  }
#undef ISSUE
#undef ISSUE_AT
#undef BSTR
  {
    const float M = m;
    float o3[3];
    reduce24(acc, o3, g);
    l += dpp<ROR8>(l); l = swap16_sum(l, l); l = swap32_sum(l, l);
#pragma unroll
    for (int i = 0; i < 3; ++i) {
      const int a = 3 * g + i;
      lds[L_QP + wave * DH + (a >> 3) * 64 + c * 8 + (a & 7)] = o3[i];
    }
    if (lane == 0) { lds[L_ML + wave * 2] = M; lds[L_ML + wave * 2 + 1] = l; }
  }
  __syncthreads();
  if (wave >= 6) return;
  const size_t p1h = WS_P1 + (size_t)(h * NSPLIT) * P1_STRIDE * 8;
  const int kq = lane >> 4;
  if (wave < 4) {
    float mw = lds[L_ML + 2 * (lane & 7)], lw = lds[L_ML + 2 * (lane & 7) + 1];
    float v[8];
    const int t = tid < DH ? tid : 0;
#pragma unroll
    for (int w = 0; w < NW; ++w) v[w] = lds[L_QP + w * DH + t];
    __builtin_amdgcn_sched_barrier(0);
    float M = fmaxf(mw, dpp<XOR1>(mw)); M = fmaxf(M, dpp<XOR2>(M)); M = fmaxf(M, dpp<HMIR>(M));
    const float f = __builtin_amdgcn_exp2f(mw - M);
    asm volatile("" : "+v"(v[0]), "+v"(v[1]), "+v"(v[2]), "+v"(v[3]), "+v"(v[4]), "+v"(v[5]), "+v"(v[6]), "+v"(v[7]), "+v"(lw));
    if (wave < 3) {
      float o = 0.f;
#pragma unroll
      for (int w = 0; w < NW; ++w)
        o = fmaf(__builtin_bit_cast(float, __builtin_amdgcn_readlane(__builtin_bit_cast(int, f), w)), v[w], o);
      st_gran(ws, p1h + ((size_t)s * P1_STRIDE + tid) * 8, tag, o);
    } else {
      float L = f * lw; L += dpp<XOR1>(L); L += dpp<XOR2>(L); L += dpp<HMIR>(L);
      if (lane < 2) st_gran(ws, p1h + ((size_t)s * P1_STRIDE + DH + lane) * 8, tag, lane ? L : M);
    }
  }
  __builtin_amdgcn_sched_barrier(0);
  u32x4 bw[6];
  float yf = 0.f;
  if (wave < 5) {
    const char* wbase = reinterpret_cast<const char*>(lds) + L_W * 4 + (8 * kq + ((lane >> 2) & 3)) * WPITCH +
                        wave * 32 + (lane & 3) * 8;
    u32x4 bf[2];
#pragma unroll
    for (int jb = 0; jb < 8; ++jb) {
      const u32x2 a = __builtin_bit_cast(u32x2, __builtin_amdgcn_ds_read_tr16_b64_v4i16(
          (lds_i16x4*)(wbase + (32 * jb) * WPITCH)));
      const u32x2 b = __builtin_bit_cast(u32x2, __builtin_amdgcn_ds_read_tr16_b64_v4i16(
          (lds_i16x4*)(wbase + (32 * jb + 4) * WPITCH)));
      if (jb < 6) bw[jb] = u32x4{a[0], a[1], b[0], b[1]};
      else bf[jb - 6] = u32x4{a[0], a[1], b[0], b[1]};
    }
    if (kq >= 2) bf[1] = u32x4{0u, 0u, 0u, 0u};
    const u32x4 a0 = *reinterpret_cast<const u32x4*>(&lds[L_H + kq * 4]);
    const u32x4 a1 = *reinterpret_cast<const u32x4*>(&lds[L_H + 16 + kq * 4]);
    f32x4 df = __builtin_amdgcn_mfma_f32_16x16x32_bf16(__builtin_bit_cast(bf16x8, a0), __builtin_bit_cast(bf16x8, bf[0]),
                                                       f32x4{0.f, 0.f, 0.f, 0.f}, 0, 0, 0);
    df = __builtin_amdgcn_mfma_f32_16x16x32_bf16(__builtin_bit_cast(bf16x8, a1), __builtin_bit_cast(bf16x8, bf[1]), df, 0, 0, 0);
    yf = lds[L_G + 1] * df[0];
    asm volatile("" : "+v"(yf), "+v"(bw[0]), "+v"(bw[1]), "+v"(bw[2]), "+v"(bw[3]), "+v"(bw[4]), "+v"(bw[5]));
  }
  const float gate = lds[L_G];
  if (wave < 3) {
    unsigned long long ga[8], gm = 0;
    bool ok[8], okm = false;
#pragma unroll
    for (int i = 0; i < 8; ++i) { ok[i] = false; ga[i] = 0; }
    for (unsigned spin = 0; spin < SPIN_CAP; ++spin) {
#pragma unroll
      for (int i = 0; i < 8; ++i)
        if (!ok[i]) ga[i] = ld_gran(ws, p1h + ((size_t)i * P1_STRIDE + tid) * 8);
      if (!okm) gm = ld_gran(ws, p1h + ((size_t)(lane & 7) * P1_STRIDE + DH + ((lane >> 3) & 1)) * 8);
      bool all = true;
#pragma unroll
      for (int i = 0; i < 8; ++i) { ok[i] = __all((unsigned)(ga[i] >> 32) == tag); all = all && ok[i]; }
      okm = __all((unsigned)(gm >> 32) == tag);
      if (all && okm) break;
      __builtin_amdgcn_s_sleep(1);
    }
    const float val = __uint_as_float((unsigned)gm);
    const float other = dpp<ROR8>(val);
    const float ms = (lane & 8) ? other : val, ls = (lane & 8) ? val : other;
    float M = fmaxf(ms, dpp<XOR1>(ms)); M = fmaxf(M, dpp<XOR2>(M)); M = fmaxf(M, dpp<HMIR>(M));
    const float wgt = __builtin_amdgcn_exp2f(ms - M);
    float L = wgt * ls; L += dpp<XOR1>(L); L += dpp<XOR2>(L); L += dpp<HMIR>(L);
    float o = 0.f;
#pragma unroll
    for (int i = 0; i < 8; ++i)
      o = fmaf(__builtin_bit_cast(float, __builtin_amdgcn_readlane(__builtin_bit_cast(int, wgt), i)),
               __uint_as_float((unsigned)ga[i]), o);
    o *= __builtin_amdgcn_rcpf(L);
    reinterpret_cast<__bf16*>(&lds[L_Z])[tid] = (__bf16)o;
  }
  __syncthreads();
  if (wave < 5) {
    u32x4 az[6];
#pragma unroll
    for (int jb = 0; jb < 6; ++jb) az[jb] = *reinterpret_cast<const u32x4*>(&lds[L_Z + jb * 16 + kq * 4]);
    f32x4 d0 = {0.f, 0.f, 0.f, 0.f};
#pragma unroll
    for (int jb = 0; jb < 6; ++jb)
      d0 = __builtin_amdgcn_mfma_f32_16x16x32_bf16(__builtin_bit_cast(bf16x8, az[jb]), __builtin_bit_cast(bf16x8, bw[jb]), d0, 0, 0, 0);
    const float y = fmaf(gate, d0[0], yf);
    if (lane < 16) st_gran(ws, WS_P2 + ((size_t)(s * HQ + h) * CPW + wave * 16 + lane) * 8, tag, y);
    return;
  }
  if (h < 20) {
    const int hh = lane >> 1, cp = (lane & 1) * 2;
    const size_t base = WS_P2 + ((size_t)(s * HQ + hh) * CPW + h * 4 + cp) * 8;
    unsigned long long g0 = 0, g1 = 0;
    for (unsigned spin = 0; spin < SPIN_CAP; ++spin) {
      g0 = ld_gran(ws, base); g1 = ld_gran(ws, base + 8);
      if (__all((unsigned)(g0 >> 32) == tag && (unsigned)(g1 >> 32) == tag)) break;
      __builtin_amdgcn_s_sleep(1);
    }
    float y0 = __uint_as_float((unsigned)g0), y1 = __uint_as_float((unsigned)g1);
    y0 += dpp<XOR2>(y0); y1 += dpp<XOR2>(y1);
    y0 += dpp<XOR1>(dpp<HMIR>(y0)); y1 += dpp<XOR1>(dpp<HMIR>(y1));
    y0 += dpp<ROR8>(y0); y1 += dpp<ROR8>(y1);
    y0 = swap16_sum(y0, y0); y1 = swap16_sum(y1, y1);
    y0 = swap32_sum(y0, y0); y1 = swap32_sum(y1, y1);
    if (lane < 2) *reinterpret_cast<unsigned*>(out + s * CPW + h * 4 + cp) = bf_pack(y0, y1);
    if (lane == 0 && h == 0 && s == 0) __hip_atomic_store((gu64*)(ws + WS_EPOCH), ep, RLX_AGENT);
  }
}

}

extern "C" void kernel_launch(void* const* d_in, const int* in_sizes, int n_in,
                              void* d_out, int out_size, void* d_ws, size_t ws_size,
                              hipStream_t stream) {
  (void)in_sizes; (void)n_in; (void)out_size; (void)ws_size;
  fused_decode<<<dim3(NWG), dim3(NT), 0, stream>>>(
      (const unsigned short*)d_in[0], (const unsigned short*)d_in[1], (const unsigned short*)d_in[2],
      (const unsigned short*)d_in[3], (const unsigned short*)d_in[4], (const unsigned short*)d_in[6],
      (const unsigned short*)d_in[7], (const unsigned short*)d_in[8], (const unsigned short*)d_in[9],
      (unsigned short*)d_out, (unsigned char*)d_ws);
}
```

```cpp
#define HOLD 0ull
#include <hip/hip_runtime.h>
#include <hip/hip_bf16.h>
#include <stdint.h>

#define RING 4
#define RING_PRO 2
#define NCTL 1
#define KV_AUX 2

namespace {

constexpr int D = 640, HQ = 32, SK = 8192, DH = 192, FF = 1536;
constexpr int NSPLIT = 8, NWG = 256, NT = 512, NW = 8;
constexpr int KEYS_WG = SK / NSPLIT;
constexpr int NB = KEYS_WG / 8 / NW;
constexpr int NQ = HQ * DH;
constexpr int CPW = D / NSPLIT;
constexpr int FPH = FF / HQ;

constexpr int NPC = DH + 2 * FPH;

constexpr size_t WS_EPOCH = 0;
constexpr int P1_STRIDE = 208;
constexpr size_t WS_P1 = 4096;
constexpr size_t WS_P2 = WS_P1 + (size_t)NWG * P1_STRIDE * 8;
constexpr size_t WS_PQ = WS_P2 + (size_t)NWG * CPW * 8;
constexpr size_t WS_END = WS_PQ + (size_t)NWG * NPC * 8;

constexpr int WPITCH = 160;
constexpr int L_W = 0;
constexpr int L_QP = L_W + 256 * WPITCH / 4;
constexpr int L_ML = L_QP + NW * DH;
constexpr int L_Q = L_ML + 16;
constexpr int L_G = L_Q + 96;
constexpr int L_H = L_G + 4;
constexpr int L_Z = L_H + 32;
constexpr int L_PART = L_Z + 96;
constexpr int L_END = L_PART + 15 * NPC;

typedef unsigned u32x4 __attribute__((ext_vector_type(4)));
typedef float f32x4 __attribute__((ext_vector_type(4)));
typedef float f32x2 __attribute__((ext_vector_type(2)));
typedef __bf16 bf16x2 __attribute__((ext_vector_type(2)));
typedef __bf16 bf16x8 __attribute__((ext_vector_type(8)));
typedef short i16x4 __attribute__((ext_vector_type(4)));
typedef unsigned u32x2 __attribute__((ext_vector_type(2)));
typedef __attribute__((address_space(3))) i16x4 lds_i16x4;
typedef __attribute__((address_space(1))) unsigned long long gu64;
typedef __attribute__((address_space(1))) unsigned gu32;
#define RLX_AGENT __ATOMIC_RELAXED, __HIP_MEMORY_SCOPE_AGENT

__device__ __forceinline__ float bf_lo(unsigned w) { return __builtin_bit_cast(float, w << 16); }
__device__ __forceinline__ float bf_hi(unsigned w) { return __builtin_bit_cast(float, w & 0xffff0000u); }
__device__ __forceinline__ float bf_round(float v) { return (float)(__bf16)v; }
__device__ __forceinline__ unsigned bf_pack(float lo, float hi) {
  bf16x2 p; p[0] = (__bf16)lo; p[1] = (__bf16)hi; return __builtin_bit_cast(unsigned, p);
}
__device__ __forceinline__ float dot2(unsigned a, unsigned b, float c) {
  return __builtin_amdgcn_fdot2_f32_bf16(__builtin_bit_cast(bf16x2, a), __builtin_bit_cast(bf16x2, b), c, false);
}
template <int CTRL> __device__ __forceinline__ float dpp(float x) {
  return __builtin_bit_cast(float, __builtin_amdgcn_mov_dpp(__builtin_bit_cast(int, x), CTRL, 0xf, 0xf, true));
}
constexpr int XOR1 = 0xB1, XOR2 = 0x4E, HMIR = 0x141, ROR8 = 0x128, ROR4 = 0x124;

__device__ __forceinline__ float swap32_sum(float a, float b) {
  auto r = __builtin_amdgcn_permlane32_swap(__float_as_uint(a), __float_as_uint(b), false, false);
  return __uint_as_float(r[0]) + __uint_as_float(r[1]);
}
__device__ __forceinline__ float swap16_sum(float a, float b) {
  auto r = __builtin_amdgcn_permlane16_swap(__float_as_uint(a), __float_as_uint(b), false, false);
  return __uint_as_float(r[0]) + __uint_as_float(r[1]);
}
__device__ __forceinline__ float swap32_max(float a) {
  auto r = __builtin_amdgcn_permlane32_swap(__float_as_uint(a), __float_as_uint(a), false, false);
  return fmaxf(__uint_as_float(r[0]), __uint_as_float(r[1]));
}
__device__ __forceinline__ float swap16_max(float a) {
  auto r = __builtin_amdgcn_permlane16_swap(__float_as_uint(a), __float_as_uint(a), false, false);
  return fmaxf(__uint_as_float(r[0]), __uint_as_float(r[1]));
}
__device__ __forceinline__ void reduce24(const float (&v)[24], float (&o)[3], int g) {
  float a[12], b[6];
#pragma unroll
  for (int i = 0; i < 12; ++i) a[i] = swap32_sum(v[i], v[i + 12]);
#pragma unroll
  for (int i = 0; i < 6; ++i) b[i] = swap16_sum(a[i], a[i + 6]);
#pragma unroll
  for (int i = 0; i < 3; ++i) {
    const float lo = b[i] + dpp<ROR8>(b[i]), hi = b[i + 3] + dpp<ROR8>(b[i + 3]);
    o[i] = (g & 1) ? hi : lo;
  }
}
__device__ __forceinline__ void st_gran(unsigned char* ws, size_t off, unsigned tag, float v) {
  __hip_atomic_store((gu64*)(ws + off), ((unsigned long long)tag << 32) | __float_as_uint(v), RLX_AGENT);
}
__device__ __forceinline__ unsigned long long ld_gran(unsigned char* ws, size_t off) {
  return __hip_atomic_load((gu64*)(ws + off), RLX_AGENT);
}
__device__ __forceinline__ __amdgpu_buffer_rsrc_t mk_rsrc(const void* p, unsigned bytes) {
  return __builtin_amdgcn_make_buffer_rsrc((void*)p, 0, bytes, 0x00020000);
}
__device__ __forceinline__ u32x4 ldw(const unsigned short* p) {
  return __builtin_nontemporal_load(reinterpret_cast<const u32x4*>(p));
}
constexpr unsigned SPIN_CAP = 1u << 18;

__global__ void __launch_bounds__(NT, 2)
fused_decode(const unsigned short* __restrict__ x, const unsigned short* __restrict__ wq,
             const unsigned short* __restrict__ wg, const unsigned short* __restrict__ wu,
             const unsigned short* __restrict__ kc, const unsigned short* __restrict__ vc, unsigned char* ws,
             const unsigned short* __restrict__ wo, const unsigned short* __restrict__ wd,
             const unsigned short* __restrict__ gw, unsigned short* __restrict__ out) {
  __shared__ __attribute__((aligned(16))) float lds[L_END];
  const unsigned long long t0 = __builtin_amdgcn_s_memrealtime();
  const int bid = blockIdx.x;
  const int h = (bid & 7) * 4 + ((bid >> 3) & 3), s = bid >> 5;
  const int tid = threadIdx.x, lane = tid & 63, wave = __builtin_amdgcn_readfirstlane(tid >> 6);
  const int g = lane >> 3, c = lane & 7;

  const int prg = tid / 36, pch = tid - prg * 36;
  u32x4 wqv[6];
  unsigned short xq[6];
  unsigned long long ep;
  {
    unsigned wofs[6], xofs[6];
    const unsigned ldb = pch < 24 ? NQ * 2 : FF * 2;
    const unsigned c0 = pch < 30 ? 24 * 16 : 30 * 16;
    const unsigned colb = pch * 16 + (pch < 24 ? (unsigned)(h * (DH * 2)) : (unsigned)(h * (FPH * 2)) - c0);
#pragma unroll
    for (int i = 0; i < 6; ++i) {
      const int r = prg + 14 * i;
      const unsigned row = s * 80 + (r < 80 ? r : 0);
      wofs[i] = row * ldb + colb;
      xofs[i] = row * 2;
    }
    asm volatile("" : "+v"(wofs[0]), "+v"(wofs[1]), "+v"(wofs[2]), "+v"(wofs[3]), "+v"(wofs[4]), "+v"(wofs[5]),
                      "+v"(xofs[0]), "+v"(xofs[4]), "+v"(xofs[5]));
    __builtin_amdgcn_sched_barrier(0);
    asm volatile("" :: "s"(x), "s"(kc), "s"(vc), "s"(wq), "s"(wo), "s"(wg), "s"(wu), "s"(wd), "s"(gw), "s"(out), "s"(ws));
    unsigned long long asel = pch < 30 ? (unsigned long long)wg : (unsigned long long)wu;
    asel = pch < 24 ? (unsigned long long)wq : asel;
#pragma unroll
    for (int i = 0; i < 6; ++i)
      wqv[i] = __builtin_nontemporal_load((const __attribute__((address_space(1))) u32x4*)(asel + wofs[i]));
    __builtin_amdgcn_sched_barrier(0);
    ep = __hip_atomic_load((gu64*)(ws + WS_EPOCH), RLX_AGENT);
#pragma unroll
    for (int i = 0; i < 6; ++i)
      xq[i] = *reinterpret_cast<const unsigned short*>(reinterpret_cast<const char*>(x) + (i < 4 ? xofs[0] + 28 * i : xofs[i]));
  }
  __builtin_amdgcn_sched_barrier(0);
  __builtin_amdgcn_s_barrier();
  __builtin_amdgcn_sched_barrier(0);

  const size_t kvbase = (size_t)h * SK * DH;
  const __amdgpu_buffer_rsrc_t rk = mk_rsrc(kc + kvbase, SK * DH * 2);
  const __amdgpu_buffer_rsrc_t rv = mk_rsrc(vc + kvbase, SK * DH * 2);
  const int kvoff = s * 24576 + wave * 3072 + g * 384 + c * 16;
#define BSTR (8 * 24576)
  u32x4 kb[RING][3], vb[RING][3];
#define ISSUE_AT(vo_, b_)                                                                                  \
  do {                                                                                                     \
    _Pragma("unroll") for (int j_ = 0; j_ < 3; ++j_)                                                       \
        kb[(b_) % RING][j_] = __builtin_amdgcn_raw_buffer_load_b128(rk, (vo_) + j_ * 128, (b_) * BSTR, KV_AUX); \
    _Pragma("unroll") for (int j_ = 0; j_ < 3; ++j_)                                                       \
        vb[(b_) % RING][j_] = __builtin_amdgcn_raw_buffer_load_b128(rv, (vo_) + j_ * 128, (b_) * BSTR, KV_AUX); \
    __builtin_amdgcn_sched_barrier(0);                                                                     \
  } while (0)
#define ISSUE(b_) ISSUE_AT(kvoff, b_)
#pragma unroll
  for (int b = 0; b < RING_PRO; ++b) ISSUE(b);
  __builtin_amdgcn_sched_barrier(0);
  {
    float qa[8];
#pragma unroll
    for (int e = 0; e < 8; ++e) qa[e] = 0.f;
#pragma unroll
    for (int i = 0; i < 6; ++i) {
      const float xv = (prg + 14 * i < 80) ? bf_lo((unsigned)xq[i]) : 0.f;
#pragma unroll
      for (int k = 0; k < 4; ++k) {
        qa[2 * k] = fmaf(xv, bf_lo(wqv[i][k]), qa[2 * k]);
        qa[2 * k + 1] = fmaf(xv, bf_hi(wqv[i][k]), qa[2 * k + 1]);
      }
    }
    *reinterpret_cast<f32x4*>(&lds[L_PART + prg * NPC + pch * 8]) = f32x4{qa[0], qa[1], qa[2], qa[3]};
    *reinterpret_cast<f32x4*>(&lds[L_PART + prg * NPC + pch * 8 + 4]) = f32x4{qa[4], qa[5], qa[6], qa[7]};
  }
  __syncthreads();
  asm volatile("" : "+v"(ep));
  ep += 1ull;
  const unsigned tag = (unsigned)ep;
  const unsigned long long tag40 = ep & 0xffffffffffull;
  const size_t pqh = WS_PQ + (size_t)(h * NSPLIT) * NPC * 8;
  if (tid < NPC) {
    float v = 0.f;
#pragma unroll
    for (int r = 0; r < 14; ++r) v += lds[L_PART + r * NPC + tid];
    const unsigned vb24 = (__float_as_uint(v) + 0x80u) >> 8;
    __hip_atomic_store((gu64*)(ws + pqh + ((size_t)s * NPC + tid) * 8), (tag40 << 24) | vb24, RLX_AGENT);
  }
  if (tid < 194) st_gran(ws, WS_P1 + ((size_t)(h * NSPLIT + s) * P1_STRIDE + tid) * 8, ~tag, 0.f);
  if (tid < 320 && (tid & 3) == 0) st_gran(ws, WS_P2 + ((size_t)(s * HQ + h) * CPW + (tid >> 2)) * 8, ~tag, 0.f);
  __builtin_amdgcn_sched_barrier(0);
  u32x4 wsl[5];
  {
    const int rg = tid / 10, ch = tid - rg * 10;
#pragma unroll
    for (int i = 0; i < 5; ++i) {
      const int r = (tid < 480) ? rg * 5 + i : 0;
      const unsigned short* p = (r < DH) ? wo + (size_t)(h * DH + r) * D : wd + (size_t)(h * FPH + (r - DH)) * D;
      wsl[i] = ldw(p + s * CPW + ch * 8);
    }
  }
  unsigned gx[5], gg[5];
  if (wave == 7) {
#pragma unroll
    for (int i = 0; i < 5; ++i) {
      gx[i] = *reinterpret_cast<const unsigned*>(x + (i * 64 + lane) * 2);
      gg[i] = *reinterpret_cast<const unsigned*>(gw + (i * 64 + lane) * 2);
    }
  }
  __builtin_amdgcn_sched_barrier(0);
  {
    const int vo = wave < NCTL ? kvoff + (1 << 24) : kvoff;
#pragma unroll
    for (int b = RING_PRO; b < RING; ++b) ISSUE_AT(vo, b);
  }
  __builtin_amdgcn_sched_barrier(0);

  {
    const int rg = tid / 10, ch = tid - rg * 10;
    const int wb = (tid < 480) ? (rg * 5 * WPITCH + ch * 16) / 4 : (240 * WPITCH + (tid - 480) * 16) / 4;
#pragma unroll
    for (int i = 0; i < 5; ++i)
      *reinterpret_cast<u32x4*>(&lds[L_W + wb + i * ((tid < 480) ? WPITCH / 4 : 128)]) = wsl[i];
  }
  if (wave == 7) {
    float acc = 0.f;
#pragma unroll
    for (int i = 0; i < 5; ++i) {
      acc += bf_round(bf_lo(gx[i]) * bf_lo(gg[i]));
      acc += bf_round(bf_hi(gx[i]) * bf_hi(gg[i]));
    }
    acc += dpp<XOR1>(acc); acc += dpp<XOR2>(acc); acc += dpp<HMIR>(acc); acc += dpp<ROR8>(acc);
    acc = swap16_sum(acc, acc); acc = swap32_sum(acc, acc);
    const float z = bf_round(acc);
    const float sg = bf_round(1.f / (1.f + __expf(-z)));
    if (lane == 0) { lds[L_G] = sg; lds[L_G + 1] = bf_round(1.f - sg); }
  }
  if (wave == 0) {
    unsigned long long gq[3][8];
#pragma unroll 1
    for (unsigned spin = 0; spin < SPIN_CAP; ++spin) {
#pragma unroll
      for (int k = 0; k < 3; ++k)
#pragma unroll
        for (int i = 0; i < 8; ++i) gq[k][i] = ld_gran(ws, pqh + ((size_t)i * NPC + k * 64 + lane) * 8);
      bool ok = true;
#pragma unroll
      for (int k = 0; k < 3; ++k)
#pragma unroll
        for (int i = 0; i < 8; ++i) ok = ok & ((gq[k][i] >> 24) == tag40);
      if (__all(ok)) break;
    }
#pragma unroll
    for (int k = 0; k < 3; ++k) {
      float q = 0.f;
#pragma unroll
      for (int i = 0; i < 8; ++i) q += __uint_as_float(((unsigned)gq[k][i]) << 8);
      const float qn = dpp<XOR1>(q);
      if ((lane & 1) == 0) reinterpret_cast<unsigned*>(lds)[L_Q + ((k * 64 + lane) >> 1)] = bf_pack(q, qn);
    }
    __syncthreads();
    const size_t pgu = pqh + (size_t)(DH + (lane < FPH ? lane : 0)) * 8;
    unsigned long long gh[16];
#pragma unroll
    for (int i = 0; i < 16; ++i) gh[i] = ld_gran(ws, pgu + ((size_t)(i >> 1) * NPC + (i & 1) * FPH) * 8);
    __builtin_amdgcn_sched_barrier(0);
#pragma unroll
    for (int b = RING_PRO; b < RING; ++b) ISSUE(b);
    float gs = 0.f, us = 0.f;
    bool okh = true;
#pragma unroll
    for (int i = 0; i < 8; ++i) {
      okh = okh & ((gh[2 * i] >> 24) == tag40) & ((gh[2 * i + 1] >> 24) == tag40);
      gs += __uint_as_float(((unsigned)gh[2 * i]) << 8);
      us += __uint_as_float(((unsigned)gh[2 * i + 1]) << 8);
    }
#pragma unroll 1
    for (unsigned spin = 0; spin < SPIN_CAP && !__all(okh); ++spin) {
#pragma unroll
      for (int i = 0; i < 16; ++i) gh[i] = ld_gran(ws, pgu + ((size_t)(i >> 1) * NPC + (i & 1) * FPH) * 8);
      gs = 0.f; us = 0.f; okh = true;
#pragma unroll
      for (int i = 0; i < 8; ++i) {
        okh = okh & ((gh[2 * i] >> 24) == tag40) & ((gh[2 * i + 1] >> 24) == tag40);
        gs += __uint_as_float(((unsigned)gh[2 * i]) << 8);
        us += __uint_as_float(((unsigned)gh[2 * i + 1]) << 8);
      }
    }
    const float hh = lane < FPH ? bf_round(gs) * bf_round(us) : 0.f;
    const float hn = dpp<XOR1>(hh);
    if ((lane & 1) == 0) reinterpret_cast<unsigned*>(lds)[L_H + (lane >> 1)] = bf_pack(hh, hn);
  } else {
    __syncthreads();
  }
  __builtin_amdgcn_sched_barrier(0);
  u32x4 qv[3];
#pragma unroll
  for (int j = 0; j < 3; ++j) qv[j] = *reinterpret_cast<const u32x4*>(&lds[L_Q + (c + 8 * j) * 4]);

  constexpr float C2 = 0.07216878364870322f * 1.4426950408889634f;
  float m = -INFINITY, l = 0.f, acc[24];
#pragma unroll
  for (int k = 0; k < 24; ++k) acc[k] = 0.f;
#pragma unroll
  for (int b = 0; b < NB; ++b) {
    const int sl = b % RING;
    float d0 = 0.f, d1 = 0.f, d2 = 0.f;
#pragma unroll
    for (int k = 0; k < 4; ++k) {
      d0 = dot2(kb[sl][0][k], qv[0][k], d0);
      d1 = dot2(kb[sl][1][k], qv[1][k], d1);
      d2 = dot2(kb[sl][2][k], qv[2][k], d2);
    }
    float sc = d0 + d1 + d2;
    sc += dpp<XOR1>(sc); sc += dpp<XOR2>(sc); sc += dpp<HMIR>(sc);
    sc *= C2;
    if (__any(sc > m)) {
      float mn = fmaxf(sc, dpp<ROR8>(sc));
      mn = swap16_max(mn); mn = swap32_max(mn);
      mn = fmaxf(mn, m);
      const float alpha = __builtin_amdgcn_exp2f(m - mn);
      m = mn;
      l *= alpha;
#pragma unroll
      for (int k = 0; k < 24; ++k) acc[k] *= alpha;
    }
    const float p = __builtin_amdgcn_exp2f(sc - m);
    l += p;
#pragma unroll
    for (int j = 0; j < 3; ++j)
#pragma unroll
      for (int k = 0; k < 4; ++k) {
        f32x2 a2 = {acc[j * 8 + 2 * k], acc[j * 8 + 2 * k + 1]};
        a2 = __builtin_elementwise_fma(f32x2{p, p}, f32x2{bf_lo(vb[sl][j][k]), bf_hi(vb[sl][j][k])}, a2);
        acc[j * 8 + 2 * k] = a2[0]; acc[j * 8 + 2 * k + 1] = a2[1];
      }
#pragma unroll
    for (int k = 0; k < 24; k += 4) asm volatile("" : "+v"(acc[k]), "+v"(acc[k + 1]), "+v"(acc[k + 2]), "+v"(acc[k + 3]));
    __builtin_amdgcn_sched_barrier(0);
    if (b + RING < NB) ISSUE(b + RING);
    __builtin_amdgcn_sched_barrier(0);
  }
#undef ISSUE
#undef ISSUE_AT
#undef BSTR
  {
    const float M = m;
    float o3[3];
    reduce24(acc, o3, g);
    l += dpp<ROR8>(l); l = swap16_sum(l, l); l = swap32_sum(l, l);
#pragma unroll
    for (int i = 0; i < 3; ++i) {
      const int a = 3 * g + i;
      lds[L_QP + wave * DH + (a >> 3) * 64 + c * 8 + (a & 7)] = o3[i];
    }
    if (lane == 0) { lds[L_ML + wave * 2] = M; lds[L_ML + wave * 2 + 1] = l; }
  }
  __syncthreads();
  if (wave >= 6) return;
  const size_t p1h = WS_P1 + (size_t)(h * NSPLIT) * P1_STRIDE * 8;
  const int kq = lane >> 4;
  if (wave < 4) {
    float mw = lds[L_ML + 2 * (lane & 7)], lw = lds[L_ML + 2 * (lane & 7) + 1];
    float v[8];
    const int t = tid < DH ? tid : 0;
#pragma unroll
    for (int w = 0; w < NW; ++w) v[w] = lds[L_QP + w * DH + t];
    __builtin_amdgcn_sched_barrier(0);
    float M = fmaxf(mw, dpp<XOR1>(mw)); M = fmaxf(M, dpp<XOR2>(M)); M = fmaxf(M, dpp<HMIR>(M));
    const float f = __builtin_amdgcn_exp2f(mw - M);
    asm volatile("" : "+v"(v[0]), "+v"(v[1]), "+v"(v[2]), "+v"(v[3]), "+v"(v[4]), "+v"(v[5]), "+v"(v[6]), "+v"(v[7]), "+v"(lw));
    if (wave < 3) {
      float o = 0.f;
#pragma unroll
      for (int w = 0; w < NW; ++w)
        o = fmaf(__builtin_bit_cast(float, __builtin_amdgcn_readlane(__builtin_bit_cast(int, f), w)), v[w], o);
      st_gran(ws, p1h + ((size_t)s * P1_STRIDE + tid) * 8, tag, o);
    } else {
      float L = f * lw; L += dpp<XOR1>(L); L += dpp<XOR2>(L); L += dpp<HMIR>(L);
      if (lane < 2) st_gran(ws, p1h + ((size_t)s * P1_STRIDE + DH + lane) * 8, tag, lane ? L : M);
    }
  }
  __builtin_amdgcn_sched_barrier(0);
  u32x4 bw[6];
  float yf = 0.f;
  if (wave < 5) {
    const char* wbase = reinterpret_cast<const char*>(lds) + L_W * 4 + (8 * kq + ((lane >> 2) & 3)) * WPITCH +
                        wave * 32 + (lane & 3) * 8;
    u32x4 bf[2];
#pragma unroll
    for (int jb = 0; jb < 8; ++jb) {
      const u32x2 a = __builtin_bit_cast(u32x2, __builtin_amdgcn_ds_read_tr16_b64_v4i16(
          (lds_i16x4*)(wbase + (32 * jb) * WPITCH)));
      const u32x2 b = __builtin_bit_cast(u32x2, __builtin_amdgcn_ds_read_tr16_b64_v4i16(
          (lds_i16x4*)(wbase + (32 * jb + 4) * WPITCH)));
      if (jb < 6) bw[jb] = u32x4{a[0], a[1], b[0], b[1]};
      else bf[jb - 6] = u32x4{a[0], a[1], b[0], b[1]};
    }
    if (kq >= 2) bf[1] = u32x4{0u, 0u, 0u, 0u};
    const u32x4 a0 = *reinterpret_cast<const u32x4*>(&lds[L_H + kq * 4]);
    const u32x4 a1 = *reinterpret_cast<const u32x4*>(&lds[L_H + 16 + kq * 4]);
    f32x4 df = __builtin_amdgcn_mfma_f32_16x16x32_bf16(__builtin_bit_cast(bf16x8, a0), __builtin_bit_cast(bf16x8, bf[0]),
                                                       f32x4{0.f, 0.f, 0.f, 0.f}, 0, 0, 0);
    df = __builtin_amdgcn_mfma_f32_16x16x32_bf16(__builtin_bit_cast(bf16x8, a1), __builtin_bit_cast(bf16x8, bf[1]), df, 0, 0, 0);
    yf = lds[L_G + 1] * df[0];
    asm volatile("" : "+v"(yf), "+v"(bw[0]), "+v"(bw[1]), "+v"(bw[2]), "+v"(bw[3]), "+v"(bw[4]), "+v"(bw[5]));
  }
  const float gate = lds[L_G];
  if (wave < 3) {
    unsigned long long ga[8], gm = 0;
    bool ok[8], okm = false;
#pragma unroll
    for (int i = 0; i < 8; ++i) { ok[i] = false; ga[i] = 0; }
    for (unsigned spin = 0; spin < SPIN_CAP; ++spin) {
#pragma unroll
      for (int i = 0; i < 8; ++i)
        if (!ok[i]) ga[i] = ld_gran(ws, p1h + ((size_t)i * P1_STRIDE + tid) * 8);
      if (!okm) gm = ld_gran(ws, p1h + ((size_t)(lane & 7) * P1_STRIDE + DH + ((lane >> 3) & 1)) * 8);
      bool all = true;
#pragma unroll
      for (int i = 0; i < 8; ++i) { ok[i] = __all((unsigned)(ga[i] >> 32) == tag); all = all && ok[i]; }
      okm = __all((unsigned)(gm >> 32) == tag);
      if (all && okm) break;
      __builtin_amdgcn_s_sleep(1);
    }
    const float val = __uint_as_float((unsigned)gm);
    const float other = dpp<ROR8>(val);
    const float ms = (lane & 8) ? other : val, ls = (lane & 8) ? val : other;
    float M = fmaxf(ms, dpp<XOR1>(ms)); M = fmaxf(M, dpp<XOR2>(M)); M = fmaxf(M, dpp<HMIR>(M));
    const float wgt = __builtin_amdgcn_exp2f(ms - M);
    float L = wgt * ls; L += dpp<XOR1>(L); L += dpp<XOR2>(L); L += dpp<HMIR>(L);
    float o = 0.f;
#pragma unroll
    for (int i = 0; i < 8; ++i)
      o = fmaf(__builtin_bit_cast(float, __builtin_amdgcn_readlane(__builtin_bit_cast(int, wgt), i)),
               __uint_as_float((unsigned)ga[i]), o);
    o *= __builtin_amdgcn_rcpf(L);
    reinterpret_cast<__bf16*>(&lds[L_Z])[tid] = (__bf16)o;
  }
  __syncthreads();
  if (wave < 5) {
    u32x4 az[6];
#pragma unroll
    for (int jb = 0; jb < 6; ++jb) az[jb] = *reinterpret_cast<const u32x4*>(&lds[L_Z + jb * 16 + kq * 4]);
    f32x4 d0 = {0.f, 0.f, 0.f, 0.f};
#pragma unroll
    for (int jb = 0; jb < 6; ++jb)
      d0 = __builtin_amdgcn_mfma_f32_16x16x32_bf16(__builtin_bit_cast(bf16x8, az[jb]), __builtin_bit_cast(bf16x8, bw[jb]), d0, 0, 0, 0);
    const float y = fmaf(gate, d0[0], yf);
    if (lane < 16) st_gran(ws, WS_P2 + ((size_t)(s * HQ + h) * CPW + wave * 16 + lane) * 8, tag, y);
    return;
  }
  if (h < 20) {
    const int hh = lane >> 1, cp = (lane & 1) * 2;
    const size_t base = WS_P2 + ((size_t)(s * HQ + hh) * CPW + h * 4 + cp) * 8;
    unsigned long long g0 = 0, g1 = 0;
    for (unsigned spin = 0; spin < SPIN_CAP; ++spin) {
      g0 = ld_gran(ws, base); g1 = ld_gran(ws, base + 8);
      if (__all((unsigned)(g0 >> 32) == tag && (unsigned)(g1 >> 32) == tag)) break;
      __builtin_amdgcn_s_sleep(1);
    }
    float y0 = __uint_as_float((unsigned)g0), y1 = __uint_as_float((unsigned)g1);
    y0 += dpp<XOR2>(y0); y1 += dpp<XOR2>(y1);
    y0 += dpp<XOR1>(dpp<HMIR>(y0)); y1 += dpp<XOR1>(dpp<HMIR>(y1));
    y0 += dpp<ROR8>(y0); y1 += dpp<ROR8>(y1);
    y0 = swap16_sum(y0, y0); y1 = swap16_sum(y1, y1);
    y0 = swap32_sum(y0, y0); y1 = swap32_sum(y1, y1);
    while (__builtin_amdgcn_s_memrealtime() - t0 < HOLD) __builtin_amdgcn_s_sleep(1);
    if (lane < 2) *reinterpret_cast<unsigned*>(out + s * CPW + h * 4 + cp) = bf_pack(y0, y1);
    if (lane == 0 && h == 0 && s == 0) __hip_atomic_store((gu64*)(ws + WS_EPOCH), ep, RLX_AGENT);
  }
}

}

extern "C" void kernel_launch(void* const* d_in, const int* in_sizes, int n_in,
                              void* d_out, int out_size, void* d_ws, size_t ws_size,
                              hipStream_t stream) {
  (void)in_sizes; (void)n_in; (void)out_size; (void)ws_size;
  fused_decode<<<dim3(NWG), dim3(NT), 0, stream>>>(
      (const unsigned short*)d_in[0], (const unsigned short*)d_in[3], (const unsigned short*)d_in[6],
      (const unsigned short*)d_in[7], (const unsigned short*)d_in[1], (const unsigned short*)d_in[2],
      (unsigned char*)d_ws, (const unsigned short*)d_in[4], (const unsigned short*)d_in[8],
      (const unsigned short*)d_in[9], (unsigned short*)d_out);
}
```

```cpp
#include <hip/hip_runtime.h>
#include <hip/hip_bf16.h>
#include <stdint.h>

#define RING 4
#define RING_PRO 2
#define GATE 6u
#define NCTL 1
#define KV_AUX 2

namespace {

constexpr int D = 640, HQ = 32, SK = 8192, DH = 192, FF = 1536;
constexpr int NSPLIT = 8, NWG = 256, NT = 512, NW = 8;
constexpr int KEYS_WG = SK / NSPLIT;
constexpr int NB = KEYS_WG / 8 / NW;
constexpr int NQ = HQ * DH;
constexpr int CPW = D / NSPLIT;
constexpr int FPH = FF / HQ;

constexpr int NPC = DH + 2 * FPH;

constexpr size_t WS_EPOCH = 0;
constexpr int P1_STRIDE = 128;
constexpr size_t WS_P1 = 4096;
constexpr size_t WS_P2 = WS_P1 + (size_t)NWG * P1_STRIDE * 8;
constexpr size_t WS_PQ = WS_P2 + (size_t)NWG * CPW * 8;
constexpr size_t WS_END = WS_PQ + (size_t)NWG * NPC * 8;

constexpr int WPITCH = 160;
constexpr int L_W = 0;
constexpr int L_QP = L_W + 256 * WPITCH / 4;
constexpr int L_C = L_QP + NW * 256;
constexpr int L_Q = L_C + 68;
constexpr int L_G = L_Q + 96;
constexpr int L_E = L_G + 4;
constexpr int APITCH = 104;
constexpr int L_H = L_E + 36;
constexpr int L_PART = L_H + 96;
constexpr int L_END = L_PART + 15 * NPC;
constexpr int L_A = L_PART;

typedef unsigned u32x4 __attribute__((ext_vector_type(4)));
typedef float f32x4 __attribute__((ext_vector_type(4)));
typedef float f32x2 __attribute__((ext_vector_type(2)));
typedef float f32x32 __attribute__((ext_vector_type(32)));
typedef __bf16 bf16x2 __attribute__((ext_vector_type(2)));
typedef __bf16 bf16x8 __attribute__((ext_vector_type(8)));
typedef short i16x4 __attribute__((ext_vector_type(4)));
typedef unsigned u32x2 __attribute__((ext_vector_type(2)));
typedef __attribute__((address_space(3))) i16x4 lds_i16x4;
typedef __attribute__((address_space(1))) unsigned long long gu64;
typedef __attribute__((address_space(1))) unsigned gu32;
#define RLX_AGENT __ATOMIC_RELAXED, __HIP_MEMORY_SCOPE_AGENT

__device__ __forceinline__ float bf_lo(unsigned w) { return __builtin_bit_cast(float, w << 16); }
__device__ __forceinline__ float bf_hi(unsigned w) { return __builtin_bit_cast(float, w & 0xffff0000u); }
__device__ __forceinline__ float bf_round(float v) { return (float)(__bf16)v; }
__device__ __forceinline__ unsigned bf_pack(float lo, float hi) {
  bf16x2 p; p[0] = (__bf16)lo; p[1] = (__bf16)hi; return __builtin_bit_cast(unsigned, p);
}
__device__ __forceinline__ float dot2(unsigned a, unsigned b, float c) {
  return __builtin_amdgcn_fdot2_f32_bf16(__builtin_bit_cast(bf16x2, a), __builtin_bit_cast(bf16x2, b), c, false);
}
template <int CTRL> __device__ __forceinline__ float dpp(float x) {
  return __builtin_bit_cast(float, __builtin_amdgcn_mov_dpp(__builtin_bit_cast(int, x), CTRL, 0xf, 0xf, true));
}
constexpr int XOR1 = 0xB1, XOR2 = 0x4E, HMIR = 0x141, ROR8 = 0x128, ROR4 = 0x124;

__device__ __forceinline__ float swap32_sum(float a, float b) {
  auto r = __builtin_amdgcn_permlane32_swap(__float_as_uint(a), __float_as_uint(b), false, false);
  return __uint_as_float(r[0]) + __uint_as_float(r[1]);
}
__device__ __forceinline__ float swap16_sum(float a, float b) {
  auto r = __builtin_amdgcn_permlane16_swap(__float_as_uint(a), __float_as_uint(b), false, false);
  return __uint_as_float(r[0]) + __uint_as_float(r[1]);
}
__device__ __forceinline__ float swap32_max(float a) {
  auto r = __builtin_amdgcn_permlane32_swap(__float_as_uint(a), __float_as_uint(a), false, false);
  return fmaxf(__uint_as_float(r[0]), __uint_as_float(r[1]));
}
__device__ __forceinline__ float swap16_max(float a) {
  auto r = __builtin_amdgcn_permlane16_swap(__float_as_uint(a), __float_as_uint(a), false, false);
  return fmaxf(__uint_as_float(r[0]), __uint_as_float(r[1]));
}
#define MAXD(r_, x_, ctl_) asm("s_nop 1\n\tv_max_f32_dpp %0, %1, %1 " ctl_ " row_mask:0xf bank_mask:0xf" : "=v"(r_) : "v"(x_))
#define MAXE(r_, x_, ctl_) asm volatile("v_max_f32_dpp %0, %1, %1 " ctl_ " row_mask:0xf bank_mask:0xf" : "=v"(r_) : "v"(x_))
#define SB() __builtin_amdgcn_sched_barrier(0)
__device__ __forceinline__ void st_gran(unsigned char* ws, size_t off, unsigned tag, float v) {
  __hip_atomic_store((gu64*)(ws + off), ((unsigned long long)tag << 32) | __float_as_uint(v), RLX_AGENT);
}
__device__ __forceinline__ unsigned long long ld_gran(unsigned char* ws, size_t off) {
  return __hip_atomic_load((gu64*)(ws + off), RLX_AGENT);
}
__device__ __forceinline__ __amdgpu_buffer_rsrc_t mk_rsrc(const void* p, unsigned bytes) {
  return __builtin_amdgcn_make_buffer_rsrc((void*)p, 0, bytes, 0x00020000);
}
__device__ __forceinline__ u32x4 ldw(const unsigned short* p) {
  return *reinterpret_cast<const u32x4*>(p);
}
constexpr unsigned SPIN_CAP = 1u << 18;

__global__ void __launch_bounds__(NT, 2)
fused_decode(const unsigned short* __restrict__ x, const unsigned short* __restrict__ wq,
             const unsigned short* __restrict__ wg, const unsigned short* __restrict__ wu,
             const unsigned short* __restrict__ kc, const unsigned short* __restrict__ vc, unsigned char* ws,
             const unsigned short* __restrict__ wo, const unsigned short* __restrict__ wd,
             const unsigned short* __restrict__ gw, unsigned short* __restrict__ out) {
  __shared__ __attribute__((aligned(16))) float lds[L_END];
  const int bid = blockIdx.x;
  const int h = (bid & 7) * 4 + ((bid >> 3) & 3), s = bid >> 5;
  const int tid = threadIdx.x, lane = tid & 63, wave = __builtin_amdgcn_readfirstlane(tid >> 6);
  const int g = lane >> 3, c = lane & 7;
  const int prg = tid / 36, pch = tid - prg * 36;
  u32x4 wqv[6];
  unsigned short xq[6];
  unsigned long long ep;
  {
    unsigned wofs[6], xofs[6];
    const unsigned ldb = pch < 24 ? NQ * 2 : FF * 2;
    const unsigned c0 = pch < 30 ? 24 * 16 : 30 * 16;
    const unsigned colb = pch * 16 + (pch < 24 ? (unsigned)(h * (DH * 2)) : (unsigned)(h * (FPH * 2)) - c0);
#pragma unroll
    for (int i = 0; i < 6; ++i) {
      const int r = prg + 14 * i;
      const unsigned row = s * 80 + (r < 80 ? r : 0);
      wofs[i] = row * ldb + colb;
      xofs[i] = row * 2;
    }
    asm volatile("" : "+v"(wofs[0]), "+v"(wofs[1]), "+v"(wofs[2]), "+v"(wofs[3]), "+v"(wofs[4]), "+v"(wofs[5]),
                      "+v"(xofs[0]), "+v"(xofs[4]), "+v"(xofs[5]));
    __builtin_amdgcn_sched_barrier(0);
    asm volatile("" :: "s"(x), "s"(kc), "s"(vc), "s"(wq), "s"(wo), "s"(wg), "s"(wu), "s"(wd), "s"(gw), "s"(out), "s"(ws));
    unsigned long long asel = pch < 30 ? (unsigned long long)wg : (unsigned long long)wu;
    asel = pch < 24 ? (unsigned long long)wq : asel;
#pragma unroll
    for (int i = 0; i < 6; ++i)
      wqv[i] = __builtin_nontemporal_load((const __attribute__((address_space(1))) u32x4*)(asel + wofs[i]));
    __builtin_amdgcn_sched_barrier(0);
    ep = __hip_atomic_load((gu64*)(ws + WS_EPOCH), RLX_AGENT);
#pragma unroll
    for (int i = 0; i < 6; ++i)
      xq[i] = *reinterpret_cast<const unsigned short*>(reinterpret_cast<const char*>(x) + (i < 4 ? xofs[0] + 28 * i : xofs[i]));
  }
  __builtin_amdgcn_sched_barrier(0);
  __builtin_amdgcn_s_barrier();
  __builtin_amdgcn_sched_barrier(0);

  const size_t kvbase = (size_t)h * SK * DH;
  const __amdgpu_buffer_rsrc_t rk = mk_rsrc(kc + kvbase, SK * DH * 2);
  const __amdgpu_buffer_rsrc_t rv = mk_rsrc(vc + kvbase, SK * DH * 2);
  const int kvoff = s * 24576 + wave * 3072 + g * 384 + c * 16;
#define BSTR (8 * 24576)
  u32x4 kb[RING][3], vb[RING][3];
#define ISSUE_AT(vo_, b_)                                                                                  \
  do {                                                                                                     \
    _Pragma("unroll") for (int j_ = 0; j_ < 3; ++j_)                                                       \
        kb[(b_) % RING][j_] = __builtin_amdgcn_raw_buffer_load_b128(rk, (vo_) + j_ * 128, (b_) * BSTR, KV_AUX); \
    _Pragma("unroll") for (int j_ = 0; j_ < 3; ++j_)                                                       \
        vb[(b_) % RING][j_] = __builtin_amdgcn_raw_buffer_load_b128(rv, (vo_) + j_ * 128, (b_) * BSTR, KV_AUX); \
    __builtin_amdgcn_sched_barrier(0);                                                                     \
  } while (0)
#define ISSUE(b_) ISSUE_AT(kvoff, b_)
#pragma unroll
  for (int b = 0; b < RING_PRO; ++b) ISSUE(b);
  __builtin_amdgcn_sched_barrier(0);
  {
    float qa[8];
#pragma unroll
    for (int e = 0; e < 8; ++e) qa[e] = 0.f;
#pragma unroll
    for (int i = 0; i < 6; ++i) {
      const float xv = (prg + 14 * i < 80) ? bf_lo((unsigned)xq[i]) : 0.f;
#pragma unroll
      for (int k = 0; k < 4; ++k) {
        qa[2 * k] = fmaf(xv, bf_lo(wqv[i][k]), qa[2 * k]);
        qa[2 * k + 1] = fmaf(xv, bf_hi(wqv[i][k]), qa[2 * k + 1]);
      }
    }
    *reinterpret_cast<f32x4*>(&lds[L_PART + prg * NPC + pch * 8]) = f32x4{qa[0], qa[1], qa[2], qa[3]};
    *reinterpret_cast<f32x4*>(&lds[L_PART + prg * NPC + pch * 8 + 4]) = f32x4{qa[4], qa[5], qa[6], qa[7]};
  }
  __syncthreads();
  asm volatile("" : "+v"(ep));
  ep += 1ull;
  const unsigned tag = (unsigned)ep;
  const unsigned long long tag40 = ep & 0xffffffffffull;
  const size_t pqh = WS_PQ + (size_t)(h * NSPLIT) * NPC * 8;
  if (tid < NPC) {
    float v = 0.f;
#pragma unroll
    for (int r = 0; r < 14; ++r) v += lds[L_PART + r * NPC + tid];
    const unsigned vb24 = (__float_as_uint(v) + 0x80u) >> 8;
    __hip_atomic_store((gu64*)(ws + pqh + ((size_t)s * NPC + tid) * 8), (tag40 << 24) | vb24, RLX_AGENT);
  }
  if (tid < 98) st_gran(ws, WS_P1 + ((size_t)(h * NSPLIT + s) * P1_STRIDE + tid) * 8, ~tag, 0.f);
  if (tid < 320 && (tid & 3) == 0) st_gran(ws, WS_P2 + ((size_t)(s * HQ + h) * CPW + (tid >> 2)) * 8, ~tag, 0.f);
  __builtin_amdgcn_sched_barrier(0);
  u32x4 wsl[5];
  {
    const int rg = tid / 10, ch = tid - rg * 10;
#pragma unroll
    for (int i = 0; i < 5; ++i) {
      const int r = (tid < 480) ? rg * 5 + i : 0;
      const unsigned short* p = (r < DH) ? wo + (size_t)(h * DH + r) * D : wd + (size_t)(h * FPH + (r - DH)) * D;
      wsl[i] = ldw(p + s * CPW + ch * 8);
    }
  }
  unsigned gx[5], gg[5];
  if (wave == 7) {
#pragma unroll
    for (int i = 0; i < 5; ++i) {
      gx[i] = *reinterpret_cast<const unsigned*>(x + (i * 64 + lane) * 2);
      gg[i] = *reinterpret_cast<const unsigned*>(gw + (i * 64 + lane) * 2);
    }
  }
  __builtin_amdgcn_sched_barrier(0);
  {
    const int vo = wave < NCTL ? kvoff + (1 << 24) : kvoff;
#pragma unroll
    for (int b = RING_PRO; b < RING; ++b) ISSUE_AT(vo, b);
  }
  __builtin_amdgcn_sched_barrier(0);

  {
    const int rg = tid / 10, ch = tid - rg * 10;
    const int wb = (tid < 480) ? (rg * 5 * WPITCH + ch * 16) / 4 : (240 * WPITCH + (tid - 480) * 16) / 4;
#pragma unroll
    for (int i = 0; i < 5; ++i)
      *reinterpret_cast<u32x4*>(&lds[L_W + wb + i * ((tid < 480) ? WPITCH / 4 : 128)]) = wsl[i];
  }
  if (wave == 7) {
    float acc = 0.f;
#pragma unroll
    for (int i = 0; i < 5; ++i) {
      acc += bf_round(bf_lo(gx[i]) * bf_lo(gg[i]));
      acc += bf_round(bf_hi(gx[i]) * bf_hi(gg[i]));
    }
    acc += dpp<XOR1>(acc); acc += dpp<XOR2>(acc); acc += dpp<HMIR>(acc); acc += dpp<ROR8>(acc);
    acc = swap16_sum(acc, acc); acc = swap32_sum(acc, acc);
    const float z = bf_round(acc);
    const float sg = bf_round(1.f / (1.f + __expf(-z)));
    if (lane == 0) { lds[L_G] = sg; lds[L_G + 1] = bf_round(1.f - sg); lds[L_C + 3] = 0.f; }
    lds[L_C + 4 + lane] = 0.f;
    lds[L_H + 32 + lane] = 0.f;
    if (lane < 4) lds[L_E + 32 + lane] = (lane & 1) ? 1.f : 0.f;
  }
  if (wave == 0) {
    unsigned long long gq[3][8];
#pragma unroll 1
    for (unsigned spin = 0; spin < SPIN_CAP; ++spin) {
#pragma unroll
      for (int k = 0; k < 3; ++k)
#pragma unroll
        for (int i = 0; i < 8; ++i) gq[k][i] = ld_gran(ws, pqh + ((size_t)i * NPC + k * 64 + lane) * 8);
      bool ok = true;
#pragma unroll
      for (int k = 0; k < 3; ++k)
#pragma unroll
        for (int i = 0; i < 8; ++i) ok = ok & ((gq[k][i] >> 24) == tag40);
      if (__all(ok)) break;
    }
#pragma unroll
    for (int k = 0; k < 3; ++k) {
      float q = 0.f;
#pragma unroll
      for (int i = 0; i < 8; ++i) q += __uint_as_float(((unsigned)gq[k][i]) << 8);
      const float qn = dpp<XOR1>(q);
      if ((lane & 1) == 0) reinterpret_cast<unsigned*>(lds)[L_Q + ((k * 64 + lane) >> 1)] = bf_pack(q, qn);
    }
    __syncthreads();
    const size_t pgu = pqh + (size_t)(DH + (lane < FPH ? lane : 0)) * 8;
    unsigned long long gh[16];
#pragma unroll
    for (int i = 0; i < 16; ++i) gh[i] = ld_gran(ws, pgu + ((size_t)(i >> 1) * NPC + (i & 1) * FPH) * 8);
    __builtin_amdgcn_sched_barrier(0);
#pragma unroll
    for (int b = RING_PRO; b < RING; ++b) ISSUE(b);
    float gs = 0.f, us = 0.f;
    bool okh = true;
#pragma unroll
    for (int i = 0; i < 8; ++i) {
      okh = okh & ((gh[2 * i] >> 24) == tag40) & ((gh[2 * i + 1] >> 24) == tag40);
      gs += __uint_as_float(((unsigned)gh[2 * i]) << 8);
      us += __uint_as_float(((unsigned)gh[2 * i + 1]) << 8);
    }
#pragma unroll 1
    for (unsigned spin = 0; spin < SPIN_CAP && !__all(okh); ++spin) {
#pragma unroll
      for (int i = 0; i < 16; ++i) gh[i] = ld_gran(ws, pgu + ((size_t)(i >> 1) * NPC + (i & 1) * FPH) * 8);
      gs = 0.f; us = 0.f; okh = true;
#pragma unroll
      for (int i = 0; i < 8; ++i) {
        okh = okh & ((gh[2 * i] >> 24) == tag40) & ((gh[2 * i + 1] >> 24) == tag40);
        gs += __uint_as_float(((unsigned)gh[2 * i]) << 8);
        us += __uint_as_float(((unsigned)gh[2 * i + 1]) << 8);
      }
    }
    const float hh = lane < FPH ? bf_round(gs) * bf_round(us) : 0.f;
    const float hn = dpp<XOR1>(hh);
    if ((lane & 1) == 0) reinterpret_cast<unsigned*>(lds)[L_H + (lane >> 1)] = bf_pack(hh, hn);
    asm volatile("" ::: "memory");
    if (lane == 0) __hip_atomic_store(reinterpret_cast<unsigned*>(&lds[L_C + 3]), 1u, __ATOMIC_RELAXED, __HIP_MEMORY_SCOPE_WORKGROUP);
  } else {
    __syncthreads();
  }
  __builtin_amdgcn_sched_barrier(0);
  u32x4 qv[3];
#pragma unroll
  for (int j = 0; j < 3; ++j) qv[j] = *reinterpret_cast<const u32x4*>(&lds[L_Q + (c + 8 * j) * 4]);

  const int kq = lane >> 4, b3 = g & 1;
  auto img = [&](int a) -> int { return (a >> 3) * 32 + c * 4 + ((a & 7) >> 1); };
  auto il0 = [&](int sp) -> unsigned { return (L_A + sp * APITCH + img(6 * kq + 4 * b3)) * 4; };
  auto il1 = [&](int sp) -> unsigned {
    const int e = lane & 1;
    return (b3 ? L_E + e * 16 + (sp & 3) * 4 + ((sp >> 2) ^ e) * 2 + (c >> 2) : L_A + sp * APITCH + img(6 * kq + 2)) * 4;
  };
  const bool junk = b3 && kq < 3;
  unsigned a_s0 = (unsigned)WS_P1 + ((h * NSPLIT + s) * P1_STRIDE + lane) * 8;
  unsigned a_s1 = (unsigned)WS_P1 + ((h * NSPLIT + s) * P1_STRIDE + (junk ? 98 : b3 ? 96 + (c >> 2) : 64 + kq * 8 + c)) * 8;
  unsigned a_o0 = il0(s), a_o1 = junk ? (L_A + 96) * 4 : il1(s);
  unsigned a_v = (L_QP + lane * 4) * 4, a_w = a_v + wave * 1024, a_m = (L_QP + c * 256 + (56 + c) * 4 + 3) * 4;
  unsigned a_c = (L_C + 4 + lane) * 4;
  u32x2 st0 = {0u, tag}, st1 = {0u, tag};
  unsigned long long mine = 0x0101010101010101ull << wave;
  asm volatile("" : "+v"(a_s0), "+v"(a_s1), "+v"(a_o0), "+v"(a_o1), "+v"(a_v), "+v"(a_w), "+v"(a_m), "+v"(a_c), "+v"(st0), "+v"(st1));
  asm volatile("" : "+s"(mine));
  constexpr float C2 = 0.07216878364870322f * 1.4426950408889634f;
  float m = -INFINITY, l = 0.f, acc[24];
#pragma unroll
  for (int k = 0; k < 24; ++k) acc[k] = 0.f;
#define SCORE(sl)                                                                                   \
    float d0 = 0.f, d1 = 0.f, d2 = 0.f;                                                             \
    _Pragma("unroll") for (int k = 0; k < 4; ++k) {                                                 \
      d0 = dot2(kb[sl][0][k], qv[0][k], d0);                                                        \
      d1 = dot2(kb[sl][1][k], qv[1][k], d1);                                                        \
      d2 = dot2(kb[sl][2][k], qv[2][k], d2);                                                        \
    }                                                                                               \
    float sc = d0 + d1 + d2;
#define ACCUM(sl)                                                                                   \
    sc += dpp<XOR1>(sc); sc += dpp<XOR2>(sc); sc += dpp<HMIR>(sc);                                  \
    sc *= C2;                                                                                       \
    if (__any(sc > m)) {                                                                            \
      float mn = fmaxf(sc, dpp<ROR8>(sc));                                                          \
      mn = swap16_max(mn); mn = swap32_max(mn);                                                     \
      mn = fmaxf(mn, m);                                                                            \
      const float alpha = __builtin_amdgcn_exp2f(m - mn);                                           \
      m = mn;                                                                                       \
      l *= alpha;                                                                                   \
      _Pragma("unroll") for (int k = 0; k < 24; ++k) acc[k] *= alpha;                               \
    }                                                                                               \
    const float p = __builtin_amdgcn_exp2f(sc - m);                                                 \
    l += p;                                                                                         \
    _Pragma("unroll") for (int j = 0; j < 3; ++j)                                                   \
      _Pragma("unroll") for (int k = 0; k < 4; ++k) {                                               \
        f32x2 a2 = {acc[j * 8 + 2 * k], acc[j * 8 + 2 * k + 1]};                                    \
        a2 = __builtin_elementwise_fma(f32x2{p, p}, f32x2{bf_lo(vb[sl][j][k]), bf_hi(vb[sl][j][k])}, a2); \
        acc[j * 8 + 2 * k] = a2[0]; acc[j * 8 + 2 * k + 1] = a2[1];                                 \
      }                                                                                             \
    _Pragma("unroll") for (int k = 0; k < 24; k += 4)                                               \
      asm volatile("" : "+v"(acc[k]), "+v"(acc[k + 1]), "+v"(acc[k + 2]), "+v"(acc[k + 3]));        \
    __builtin_amdgcn_sched_barrier(0);
#pragma unroll
  for (int b = 0; b < NB; ++b) {
    const int sl = b % RING;
    SCORE(sl)
    ACCUM(sl)
    if (b + RING < NB) ISSUE(b + RING);
    __builtin_amdgcn_sched_barrier(0);
  }
#undef ISSUE
#undef ISSUE_AT
#undef BSTR

  char* const ldsc = reinterpret_cast<char*>(lds);
  unsigned* const cnt = reinterpret_cast<unsigned*>(ldsc + a_c);
  unsigned tk;
  {
    asm volatile("" : "+v"(l));
    const unsigned c0 = __hip_atomic_load(cnt, __ATOMIC_RELAXED, __HIP_MEMORY_SCOPE_WORKGROUP);
    asm volatile("" ::: "memory");
    float mw = *reinterpret_cast<const float*>(ldsc + a_m);
    SB();
    f32x32 V;
#define RDV()                                                                                      \
    _Pragma("unroll") for (int w = 0; w < NW; ++w) {                                               \
      const f32x4 t = *reinterpret_cast<const f32x4*>(ldsc + a_v + w * 1024);                      \
      V[4 * w] = t[0]; V[4 * w + 1] = t[1]; V[4 * w + 2] = t[2]; V[4 * w + 3] = t[3];              \
    }
    RDV()
    SB();
    f32x2 a2[6], b2[3];
    auto lv1 = [&](int i) {
      auto r0 = __builtin_amdgcn_permlane32_swap(__float_as_uint(acc[2 * i]), __float_as_uint(acc[2 * i + 12]), false, false);
      auto r1 = __builtin_amdgcn_permlane32_swap(__float_as_uint(acc[2 * i + 1]), __float_as_uint(acc[2 * i + 13]), false, false);
      a2[i] = f32x2{__uint_as_float(r0[0]), __uint_as_float(r1[0])} + f32x2{__uint_as_float(r0[1]), __uint_as_float(r1[1])};
    };
    auto lv2 = [&](int i) {
      auto r0 = __builtin_amdgcn_permlane16_swap(__float_as_uint(a2[i][0]), __float_as_uint(a2[i + 3][0]), false, false);
      auto r1 = __builtin_amdgcn_permlane16_swap(__float_as_uint(a2[i][1]), __float_as_uint(a2[i + 3][1]), false, false);
      b2[i] = f32x2{__uint_as_float(r0[0]), __uint_as_float(r1[0])} + f32x2{__uint_as_float(r0[1]), __uint_as_float(r1[1])};
    };
    asm volatile("v_add_f32_dpp %0, %0, %0 row_ror:8 row_mask:0xf bank_mask:0xf" : "+v"(l)); SB();
    lv1(0); lv1(3); SB();
    asm volatile("v_add_f32_dpp %0, %0, %0 row_bcast:15 row_mask:0xa bank_mask:0xf" : "+v"(l)); SB();
    lv1(1); lv1(4); SB();
    asm volatile("v_add_f32_dpp %0, %0, %0 row_bcast:31 row_mask:0xc bank_mask:0xf" : "+v"(l)); SB();
    lv1(2); lv1(5); SB();
    asm volatile("v_cndmask_b32_e64 %0, %0, %1, %2" : "+v"(mw) : "v"(m), "s"(mine)); SB();
    float M;
    lv2(0); SB(); MAXE(M, mw, "quad_perm:[1,0,3,2]"); SB();
    lv2(1); SB(); MAXE(M, M, "quad_perm:[2,3,0,1]"); SB();
    lv2(2); SB(); MAXE(M, M, "row_half_mirror"); SB();
    tk = __builtin_amdgcn_readfirstlane(c0); SB();
    float f = __builtin_amdgcn_exp2f(mw - M); SB();
    f32x4 own;
    own[2] = l; own[3] = m;
    asm volatile("v_add_f32_dpp %0, %4, %4 row_ror:8 row_mask:0xf bank_mask:0x3\n\t"
                 "v_add_f32_dpp %1, %5, %5 row_ror:8 row_mask:0xf bank_mask:0x3\n\t"
                 "v_add_f32_dpp %2, %6, %6 row_ror:8 row_mask:0xf bank_mask:0x3\n\t"
                 "v_add_f32_dpp %3, %7, %7 row_ror:8 row_mask:0xf bank_mask:0x3\n\t"
                 "v_add_f32_dpp %0, %8, %8 row_ror:8 row_mask:0xf bank_mask:0xc\n\t"
                 "v_add_f32_dpp %1, %9, %9 row_ror:8 row_mask:0xf bank_mask:0xc"
                 : "=&v"(own[0]), "=&v"(own[1]), "+&v"(own[2]), "+&v"(own[3])
                 : "v"(b2[0][0]), "v"(b2[0][1]), "v"(b2[1][0]), "v"(b2[1][1]), "v"(b2[2][0]), "v"(b2[2][1]));
    SB();
    asm volatile("" : "+v"(V));
    if (tk != NW - 1) {
      *reinterpret_cast<f32x4*>(ldsc + a_w) = own;
      asm volatile("" ::: "memory");
      const unsigned tv = __builtin_amdgcn_atomic_inc32(cnt, a_c, __ATOMIC_RELAXED, "workgroup");
      asm volatile("" ::: "memory");
      mw = *reinterpret_cast<const float*>(ldsc + a_m);
      SB();
      RDV()
      SB();
      tk = __builtin_amdgcn_readfirstlane(tv);
      MAXD(M, mw, "quad_perm:[1,0,3,2]"); MAXD(M, M, "quad_perm:[2,3,0,1]"); MAXD(M, M, "row_half_mirror");
      f = __builtin_amdgcn_exp2f(mw - M);
    }
    float fw[NW];
#pragma unroll
    for (int w = 0; w < NW; ++w) fw[w] = __builtin_bit_cast(float, __builtin_amdgcn_readlane(__builtin_bit_cast(int, f), w));
    SB();
#pragma unroll
    for (int i = 0; i < 4; ++i) V[wave * 4 + i] = own[i];
    f32x2 o0 = {0.f, 0.f}, o1 = {0.f, 0.f};
#pragma unroll
    for (int w = 0; w < NW; ++w) {
      o1 = __builtin_elementwise_fma(f32x2{fw[w], fw[w]}, f32x2{V[4 * w + 2], V[4 * w + 3]}, o1);
      o0 = __builtin_elementwise_fma(f32x2{fw[w], fw[w]}, f32x2{V[4 * w], V[4 * w + 1]}, o0);
    }
    SB();
    st1[0] = bf_pack(o1[0], o1[1]); SB();
    st0[0] = bf_pack(o0[0], o0[1]); SB();
    asm volatile("v_mov_b32_dpp %0, %1 quad_perm:[0,1,2,3] row_mask:0x8 bank_mask:0x4\n\t"
                 "v_mov_b32_dpp %0, %2 quad_perm:[0,1,2,3] row_mask:0x8 bank_mask:0x8"
                 : "+v"(st1[0]) : "v"(M), "v"(o1[0]));
    asm volatile("" : "+v"(st0), "+v"(st1));
    if (tk == NW - 1) {
      __hip_atomic_store((gu64*)(ws + a_s0), __builtin_bit_cast(unsigned long long, st0), RLX_AGENT);
      __hip_atomic_store((gu64*)(ws + a_s1), __builtin_bit_cast(unsigned long long, st1), RLX_AGENT);
      *reinterpret_cast<unsigned*>(ldsc + a_o0) = st0[0];
      *reinterpret_cast<unsigned*>(ldsc + a_o1) = st1[0];
    }
  }
  const char* wbase = ldsc + L_W * 4 + (8 * kq + ((lane >> 2) & 3)) * WPITCH + (wave < 5 ? wave : 0) * 32 + (lane & 3) * 8;
  auto wfrag = [&](int jb) -> u32x4 {
    const u32x2 a = __builtin_bit_cast(u32x2, __builtin_amdgcn_ds_read_tr16_b64_v4i16((lds_i16x4*)(wbase + (32 * jb) * WPITCH)));
    const u32x2 b = __builtin_bit_cast(u32x2, __builtin_amdgcn_ds_read_tr16_b64_v4i16((lds_i16x4*)(wbase + (32 * jb + 4) * WPITCH)));
    return u32x4{a[0], a[1], b[0], b[1]};
  };
  u32x4 bw[6];
  bw[0] = wfrag(6); bw[1] = wfrag(7);
#pragma unroll
  for (int jb = 2; jb < 6; ++jb) bw[jb] = wfrag(jb);
  if (kq >= 2) bw[1] = u32x4{0u, 0u, 0u, 0u};
  asm volatile("" : "+v"(bw[0]), "+v"(bw[1]), "+v"(bw[2]), "+v"(bw[3]), "+v"(bw[4]), "+v"(bw[5]));
  const bool poller = tk < NW - 1;
  const int src = (s + 1 + (int)tk) & 7;
  const unsigned pob = (unsigned)WS_P1 + (h * NSPLIT + src) * P1_STRIDE * 8;
  const unsigned po0 = pob + lane * 8, po1 = pob + (b3 ? 96 + (c >> 2) : 64 + kq * 8 + c) * 8;
  unsigned a_w0 = il0(src), a_w1 = il1(src);
  unsigned long long g0 = 0, g1 = 0;
  if (poller) {
#pragma unroll 1
    for (;;) {
      const unsigned cf = __hip_atomic_load(reinterpret_cast<unsigned*>(&lds[L_C + 3]), __ATOMIC_RELAXED, __HIP_MEMORY_SCOPE_WORKGROUP);
      const unsigned cv = __hip_atomic_load(cnt, __ATOMIC_RELAXED, __HIP_MEMORY_SCOPE_WORKGROUP);
      if (__builtin_amdgcn_readfirstlane(cv * cf) >= GATE) break;
      __builtin_amdgcn_s_sleep(2);
    }
    asm volatile("" ::: "memory");
    g0 = ld_gran(ws, po0); g1 = ld_gran(ws, po1);
  }
  asm volatile("" : "+v"(a_w0), "+v"(a_w1));
  SB();
  auto fetch = [&]() {
    SB();
    if (poller) {
#pragma unroll 1
      for (unsigned spin = 0; spin < SPIN_CAP; ++spin) {
        if (__builtin_amdgcn_ballot_w64(((unsigned)(g0 >> 32) != tag) | ((unsigned)(g1 >> 32) != tag)) == 0) break;
        g0 = ld_gran(ws, po0); g1 = ld_gran(ws, po1);
      }
      *reinterpret_cast<unsigned*>(ldsc + a_w0) = (unsigned)g0;
      *reinterpret_cast<unsigned*>(ldsc + a_w1) = (unsigned)g1;
    }
  };
  if (wave < 5) {
    float gate = lds[L_G + 1];
    f32x2 y2 = {0.f, 0.f};
    unsigned a_az = (L_H + kq * 4) * 4, a_e = (L_E + 32) * 4;
    unsigned a_p2 = (unsigned)WS_P2 + ((s * HQ + h) * CPW + wave * 16 + (lane & 15)) * 8;
    unsigned tg = ~tag;
    int second = 0;
#define MF(jb) d0 = __builtin_amdgcn_mfma_f32_16x16x32_bf16(__builtin_bit_cast(bf16x8, az[jb]), __builtin_bit_cast(bf16x8, bw[jb]), d0, 0, 0, 0); SB()
#pragma unroll 1
    for (;;) {
      asm volatile("s_cmp_eq_u32 %0, 0\n\ts_cbranch_scc1 1f\n\ts_waitcnt lgkmcnt(0)\n\ts_barrier\n1:"
                   : "+s"(second), "+v"(a_az), "+v"(a_e), "+v"(a_p2), "+v"(y2), "+v"(gate), "+v"(tg) :: "scc", "memory");
      u32x4 az[6];
      const f32x4 e = *reinterpret_cast<const f32x4*>(ldsc + a_e);
      SB();
#pragma unroll
      for (int jb = 0; jb < 6; ++jb) az[jb] = *reinterpret_cast<const u32x4*>(ldsc + a_az + jb * 64);
      SB();
      f32x4 d0 = {0.f, 0.f, 0.f, 0.f};
      float M;
      asm("v_max_f32 %0, %1, %2\n\ts_nop 1\n\tv_max_f32_dpp %0, %0, %0 quad_perm:[1,0,3,2] row_mask:0xf bank_mask:0xf"
          : "=v"(M) : "v"(e[0]), "v"(e[2]));
      SB();
      MF(0);
      MAXD(M, M, "quad_perm:[2,3,0,1]");
      float w1 = e[0] - M, w2 = e[2] - M; SB();
      MF(1);
      w1 = __builtin_amdgcn_exp2f(w1); w2 = __builtin_amdgcn_exp2f(w2); SB();
      MF(2);
      float L = fmaf(w2, e[3], w1 * e[1]);
      w1 *= gate; SB();
      MF(3);
      L += dpp<XOR1>(L); L += dpp<XOR2>(L); SB();
      MF(4);
      const float scl = __builtin_amdgcn_rcpf(L); SB();
      MF(5);
      f32x2 wa = {dpp<0x00>(w1) * scl, dpp<0x55>(w1) * scl}, wb = {dpp<0xAA>(w1) * scl, dpp<0xFF>(w1) * scl};
      asm volatile("" : "+v"(wa), "+v"(wb)); SB();
      f32x2 t2 = __builtin_elementwise_fma(wa, f32x2{d0[0], d0[1]}, y2);
      t2 = __builtin_elementwise_fma(wb, f32x2{d0[2], d0[3]}, t2);
      t2 = t2 + f32x2{t2[1], t2[0]};
      asm volatile("" : "+v"(t2));
      const float y = swap16_sum(t2[0], t2[1]);
      if (second) st_gran(ws, a_p2, tg, y);
      if (second) break;
      SB();
      bw[0] = wfrag(0); bw[1] = wfrag(1);
      gate = lds[L_G];
      y2 = f32x2{0.25f * y, 0.25f * y};
      a_az = (L_A + c * APITCH + kq * 4) * 4; a_e = (L_E + (kq & 1) * 16 + (lane & 3) * 4) * 4;
      tg = tag;
      asm volatile("" : "+v"(a_az), "+v"(a_e), "+v"(y2), "+v"(gate), "+v"(bw[0]), "+v"(bw[1]), "+v"(tg));
      fetch();
      second = 16;
    }
    return;
  }
  fetch();
  __syncthreads();
  if (h < 20 && wave == 5) {
    const unsigned base = (unsigned)WS_P2 + ((s * HQ + (lane >> 2)) * CPW + h * 4 + (lane & 3)) * 8;
    unsigned po = (s * CPW + h * 4 + (lane & 3)) * 2;
    asm volatile("" : "+v"(po));
#pragma unroll 1
    for (unsigned spin = 0; spin < SPIN_CAP; ++spin) {
      g0 = ld_gran(ws, base); g1 = ld_gran(ws, base + 16 * CPW * 8);
      if (__builtin_amdgcn_ballot_w64(((unsigned)(g0 >> 32) != tag) | ((unsigned)(g1 >> 32) != tag)) == 0) break;
    }
    float y = __uint_as_float((unsigned)g0) + __uint_as_float((unsigned)g1);
    y += dpp<ROR4>(y); y += dpp<ROR8>(y);
    y = swap16_sum(y, y); y = swap32_sum(y, y);
    *reinterpret_cast<unsigned short*>(reinterpret_cast<char*>(out) + po) = __builtin_bit_cast(unsigned short, (__bf16)y);
    if (lane == 0 && h == 0 && s == 0) __hip_atomic_store((gu64*)(ws + WS_EPOCH), ep, RLX_AGENT);
  }
}

}

extern "C" void kernel_launch(void* const* d_in, const int* in_sizes, int n_in,
                              void* d_out, int out_size, void* d_ws, size_t ws_size,
                              hipStream_t stream) {
  (void)in_sizes; (void)n_in; (void)out_size; (void)ws_size;
  fused_decode<<<dim3(NWG), dim3(NT), 0, stream>>>(
      (const unsigned short*)d_in[0], (const unsigned short*)d_in[3], (const unsigned short*)d_in[6],
      (const unsigned short*)d_in[7], (const unsigned short*)d_in[1], (const unsigned short*)d_in[2],
      (unsigned char*)d_ws, (const unsigned short*)d_in[4], (const unsigned short*)d_in[8],
      (const unsigned short*)d_in[9], (unsigned short*)d_out);
}
```
